```python
import torch

def _softmax(x, axis=-1):
    return torch.softmax(x, dim=axis)

def _gelu(x):
    return torch.nn.functional.gelu(x, approximate='tanh')

def _silu(x):
    return torch.nn.functional.silu(x)

def _sigmoid(x):
    return torch.sigmoid(x)

def _layernorm(x, axis=-1, eps=1e-05):
    m = x.mean(dim=axis, keepdim=True)
    v = x.var(dim=axis, keepdim=True, unbiased=False)
    return (x - m) / torch.sqrt(v + eps)

def _scatter_add(tgt, idx, src, axis=0):
    return tgt.clone().index_add_(axis, idx, src)

def _segment_sum(src, seg, n):
    out = torch.zeros((n,) + src.shape[1:], dtype=src.dtype)
    return out.index_add_(0, seg, src)

def _pad(x, pw):
    flat = tuple((v for pair in reversed(pw) for v in pair))
    return torch.nn.functional.pad(x, flat)
kernel_name = 'adaLN_cond__mha_block_cached__mixer_token_mlp__decode'

def setup_inputs():

    _w = lambda a: (lambda t: t.to(torch.bfloat16) if t.is_floating_point() else t)(torch.as_tensor(a))

    def _A_setup():
        B, Sq, D = (1, 1, 512)
        H, Sk, Dh = (32, 16384, 128)
        return {'x': _w((lambda *s: torch.randn(*s, dtype=torch.bfloat16) * 0.05)(B, Sq, D)), 'k_cache': _w((lambda *s: torch.randn(*s, dtype=torch.bfloat16) * 1.6)(B, H, Sk, Dh)), 'v_cache': _w((lambda *s: torch.randn(*s, dtype=torch.bfloat16) * 1.6)(B, H, Sk, Dh)), 'wq': _w((lambda *s: torch.randn(*s, dtype=torch.bfloat16) * 1.6)(D, H * Dh) * D ** (-0.5)), 'wo': _w((lambda *s: torch.randn(*s, dtype=torch.bfloat16) * 1.6)(H * Dh, D) * (H * Dh) ** (-0.5))}
    _A_in = _A_setup()

    def _B_setup():
        B, S, D, F = (1, 1, 512, 1376)
        return {'x': _w((lambda *s: torch.randn(*s, dtype=torch.bfloat16) * 0.05)(B, S, D)), 'w1': _w((lambda *s: torch.randn(*s, dtype=torch.bfloat16) * 1.6)(S, F) * S ** (-0.5)), 'b1': _w((lambda *s: torch.randn(*s, dtype=torch.bfloat16) * 1.6)(F) * 0.02), 'w2': _w((lambda *s: torch.randn(*s, dtype=torch.bfloat16) * 1.6)(F, S) * F ** (-0.5)), 'b2': _w((lambda *s: torch.randn(*s, dtype=torch.bfloat16) * 1.6)(S) * 0.02)}
    _B_in = _B_setup()
    return {'A_x': _A_in['x'], 'A_k_cache': _A_in['k_cache'], 'A_v_cache': _A_in['v_cache'], 'A_wq': _A_in['wq'], 'A_wo': _A_in['wo'], 'B_x': _B_in['x'], 'B_w1': _B_in['w1'], 'B_b1': _B_in['b1'], 'B_w2': _B_in['w2'], 'B_b2': _B_in['b2'], 'c': _w((lambda *s: torch.randn(*s, dtype=torch.bfloat16) * 0.05)(1, 512) * 8.0), 'w_mod': _w((lambda *s: torch.randn(*s, dtype=torch.bfloat16) * 0.05)(512, 6 * 512) * 512 ** (-0.5) * 8.0)}

def reference(A_x, A_k_cache, A_v_cache, A_wq, A_wo, B_x, B_w1, B_b1, B_w2, B_b2, c, w_mod):

    def _A_ref(x, k_cache, v_cache, wq, wo):
        B, Sq, D = x.shape
        H, Dh = (32, 128)
        q = (lambda x, *d: x.permute(*d))((x @ wq).reshape(B, Sq, H, Dh), 0, 2, 1, 3)
        scores = torch.einsum('bhqd,bhkd->bhqk', q, k_cache) * Dh ** (-0.5)
        p = _softmax(scores, axis=-1)
        o = torch.einsum('bhqk,bhkd->bhqd', p, v_cache)
        return (lambda x, *d: x.permute(*d))(o, 0, 2, 1, 3).reshape(B, Sq, H * Dh) @ wo

    def _B_ref(x, w1, b1, w2, b2):
        B, S, D = x.shape
        h = _layernorm(x, axis=-1)
        u = torch.einsum('bsd,sf->bdf', h, w1) + b1
        u = _gelu(u)
        y = torch.einsum('bdf,fs->bds', u, w2) + b2
        return (lambda x, *d: x.permute(*d))(y, 0, 2, 1)
    x = A_x
    mod = (_silu(c) @ w_mod).reshape(c.shape[0], 6, 1, -1)
    sa, ca, ga, sb, cb, gb = (mod[:, 0].clone(), mod[:, 1].clone(), mod[:, 2].clone(), mod[:, 3].clone(), mod[:, 4].clone(), mod[:, 5].clone())
    h = _layernorm(x, axis=-1) * (1 + ca) + sa
    x = x + ga * _A_ref(h, A_k_cache, A_v_cache, A_wq, A_wo)
    h = _layernorm(x, axis=-1) * (1 + cb) + sb
    x = x + gb * _B_ref(h, B_w1, B_b1, B_w2, B_b2)
    return x
```

```cpp
#include <hip/hip_runtime.h>
#include <hip/hip_bf16.h>
#include <stdint.h>

typedef unsigned u32x4 __attribute__((ext_vector_type(4)));
typedef float f32x4 __attribute__((ext_vector_type(4)));
typedef __bf16 bf16x2 __attribute__((ext_vector_type(2)));

constexpr int D = 512, H = 32, SK = 16384, DH = 128, F = 1376;
constexpr int NSPLIT = 8;

__device__ __forceinline__ float bflo(unsigned u) { return __builtin_bit_cast(float, u << 16); }
__device__ __forceinline__ float bfhi(unsigned u) { return __builtin_bit_cast(float, u & 0xffff0000u); }
__device__ __forceinline__ float bf2f(unsigned short b) { return __builtin_bit_cast(float, (unsigned)b << 16); }
__device__ __forceinline__ float dot2(unsigned k, unsigned q, float acc) {
  return __builtin_amdgcn_fdot2_f32_bf16(__builtin_bit_cast(bf16x2, k), __builtin_bit_cast(bf16x2, q), acc, false);
}
__device__ __forceinline__ float wave_sum(float v) {
  for (int o = 32; o; o >>= 1) v += __shfl_xor(v, o);
  return v;
}
__device__ __forceinline__ float wave_max(float v) {
  for (int o = 32; o; o >>= 1) v = fmaxf(v, __shfl_xor(v, o));
  return v;
}

__device__ __forceinline__ void gemv64(const float* vec_lds, const unsigned short* W, int ldw, int col0, float* red, float* out) {
  const int t = threadIdx.x, cg = t & 7, rg = t >> 3;
  u32x4 w[16];
#pragma unroll
  for (int i = 0; i < 16; ++i) w[i] = *(const u32x4*)(W + (size_t)(rg * 16 + i) * ldw + col0 + cg * 8);
  float a[8] = {0, 0, 0, 0, 0, 0, 0, 0};
#pragma unroll
  for (int i = 0; i < 16; ++i) {
    const float s = vec_lds[rg * 16 + i];
#pragma unroll
    for (int j = 0; j < 4; ++j) { a[2 * j] = fmaf(s, bflo(w[i][j]), a[2 * j]); a[2 * j + 1] = fmaf(s, bfhi(w[i][j]), a[2 * j + 1]); }
  }
#pragma unroll
  for (int j = 0; j < 8; ++j) red[rg * 64 + cg * 8 + j] = a[j];
  __syncthreads();
  if (t < 64) {
    float s = 0.f;
#pragma unroll
    for (int r = 0; r < 32; ++r) s += red[r * 64 + t];
    out[col0 + t] = s;
  }
}

__global__ void __launch_bounds__(256) k_mod(const unsigned short* c, const unsigned short* wmod, float* mod) {
  __shared__ float vec[512];
  __shared__ float red[32 * 64];
  for (int i = threadIdx.x; i < 512; i += 256) { float v = bf2f(c[i]); vec[i] = v / (1.f + __expf(-v)); }
  __syncthreads();
  gemv64(vec, wmod, 3072, blockIdx.x * 64, red, mod);
}

__device__ __forceinline__ void block_ln_stats(float v0, float v1, float* sred, float& mean, float& rstd) {
  float s = wave_sum(v0 + v1);
  if ((threadIdx.x & 63) == 0) sred[threadIdx.x >> 6] = s;
  __syncthreads();
  mean = (sred[0] + sred[1] + sred[2] + sred[3]) * (1.f / 512.f);
  __syncthreads();
  float d0 = v0 - mean, d1 = v1 - mean;
  float q = wave_sum(d0 * d0 + d1 * d1);
  if ((threadIdx.x & 63) == 0) sred[threadIdx.x >> 6] = q;
  __syncthreads();
  float var = (sred[0] + sred[1] + sred[2] + sred[3]) * (1.f / 512.f);
  rstd = 1.f / sqrtf(var + 1e-5f);
  __syncthreads();
}

__global__ void __launch_bounds__(256) k_q(const unsigned short* x, const float* mod, const unsigned short* wq, float* q) {
  __shared__ float vec[512];
  __shared__ float red[32 * 64];
  __shared__ float sred[4];
  const int t = threadIdx.x;
  float v0 = bf2f(x[t]), v1 = bf2f(x[t + 256]);
  float mean, rstd;
  block_ln_stats(v0, v1, sred, mean, rstd);
  vec[t] = (v0 - mean) * rstd * (1.f + mod[512 + t]) + mod[t];
  vec[t + 256] = (v1 - mean) * rstd * (1.f + mod[512 + t + 256]) + mod[t + 256];
  __syncthreads();
  gemv64(vec, wq, 4096, blockIdx.x * 64, red, q);
}

__global__ void __launch_bounds__(512, 2) k_attn(const unsigned short* K, const unsigned short* V, const float* q, float* pml, float* pacc) {
  __shared__ float lds[8 * 132];
  const int h = blockIdx.x >> 3, s = blockIdx.x & 7;
  const int t = threadIdx.x, lane = t & 63, w = t >> 6, r = lane >> 4, c = lane & 15;
  float qf[8];
#pragma unroll
  for (int j = 0; j < 8; ++j) qf[j] = q[h * 128 + c * 8 + j] * (0.08838834764831845f * 1.4426950408889634f);
  const size_t base = ((size_t)h * SK + s * 2048 + w * 256) * DH + lane * 8;
  const u32x4* kp = (const u32x4*)(K + base);
  const u32x4* vp = (const u32x4*)(V + base);
  float m = -INFINITY, l = 0.f, acc[8] = {0, 0, 0, 0, 0, 0, 0, 0};
  for (int it = 0; it < 8; ++it) {
    u32x4 kk[8], vv[8];
#pragma unroll
    for (int i = 0; i < 8; ++i) { kk[i] = __builtin_nontemporal_load(kp + (it * 8 + i) * 64); vv[i] = __builtin_nontemporal_load(vp + (it * 8 + i) * 64); }
    float sc[8];
#pragma unroll
    for (int i = 0; i < 8; ++i) {
      float d = 0.f;
#pragma unroll
      for (int j = 0; j < 4; ++j) { d = fmaf(bflo(kk[i][j]), qf[2 * j], d); d = fmaf(bfhi(kk[i][j]), qf[2 * j + 1], d); }
      d += __shfl_xor(d, 1); d += __shfl_xor(d, 2); d += __shfl_xor(d, 4); d += __shfl_xor(d, 8);
      sc[i] = d;
    }
    float mx = sc[0];
#pragma unroll
    for (int i = 1; i < 8; ++i) mx = fmaxf(mx, sc[i]);
    const float mn = fmaxf(m, mx);
    const float al = exp2f(m - mn);
    l *= al;
#pragma unroll
    for (int j = 0; j < 8; ++j) acc[j] *= al;
    m = mn;
#pragma unroll
    for (int i = 0; i < 8; ++i) {
      const float p = exp2f(sc[i] - m);
      l += p;
#pragma unroll
      for (int j = 0; j < 4; ++j) { acc[2 * j] = fmaf(p, bflo(vv[i][j]), acc[2 * j]); acc[2 * j + 1] = fmaf(p, bfhi(vv[i][j]), acc[2 * j + 1]); }
    }
  }
  float M = fmaxf(m, __shfl_xor(m, 16)); M = fmaxf(M, __shfl_xor(M, 32));
  const float wt = exp2f(m - M);
  l *= wt; l += __shfl_xor(l, 16); l += __shfl_xor(l, 32);
#pragma unroll
  for (int j = 0; j < 8; ++j) { float a = acc[j] * wt; a += __shfl_xor(a, 16); a += __shfl_xor(a, 32); acc[j] = a; }
  if (r == 0) {
#pragma unroll
    for (int j = 0; j < 8; ++j) lds[w * 132 + 4 + c * 8 + j] = acc[j];
    if (c == 0) { lds[w * 132] = M; lds[w * 132 + 1] = l; }
  }
  __syncthreads();
  if (t < 128) {
    float MM = -INFINITY;
#pragma unroll
    for (int i = 0; i < 8; ++i) MM = fmaxf(MM, lds[i * 132]);
    float L = 0.f, o = 0.f;
#pragma unroll
    for (int i = 0; i < 8; ++i) { const float ww = exp2f(lds[i * 132] - MM); L = fmaf(ww, lds[i * 132 + 1], L); o = fmaf(ww, lds[i * 132 + 4 + t], o); }
    pacc[blockIdx.x * 128 + t] = o;
    if (t == 0) { pml[blockIdx.x * 2] = MM; pml[blockIdx.x * 2 + 1] = L; }
  }
}

__global__ void __launch_bounds__(256) k_wo(const float* pml, const float* pacc, const unsigned short* wo, float* z) {
  __shared__ float o[128];
  __shared__ float red[32 * 64];
  const int h = blockIdx.x >> 3, s = blockIdx.x & 7, t = threadIdx.x;
  const int cg = t & 7, rg = t >> 3;
  u32x4 w[4];
#pragma unroll
  for (int i = 0; i < 4; ++i) w[i] = *(const u32x4*)(wo + (size_t)(h * 128 + rg * 4 + i) * 512 + s * 64 + cg * 8);
  if (t < 128) {
    float MM = -INFINITY;
#pragma unroll
    for (int i = 0; i < 8; ++i) MM = fmaxf(MM, pml[(h * 8 + i) * 2]);
    float L = 0.f, a = 0.f;
#pragma unroll
    for (int i = 0; i < 8; ++i) { const float ww = exp2f(pml[(h * 8 + i) * 2] - MM); L = fmaf(ww, pml[(h * 8 + i) * 2 + 1], L); a = fmaf(ww, pacc[(h * 8 + i) * 128 + t], a); }
    o[t] = a / L;
  }
  __syncthreads();
  float a[8] = {0, 0, 0, 0, 0, 0, 0, 0};
#pragma unroll
  for (int i = 0; i < 4; ++i) {
    const float sv = o[rg * 4 + i];
#pragma unroll
    for (int j = 0; j < 4; ++j) { a[2 * j] = fmaf(sv, bflo(w[i][j]), a[2 * j]); a[2 * j + 1] = fmaf(sv, bfhi(w[i][j]), a[2 * j + 1]); }
  }
#pragma unroll
  for (int j = 0; j < 8; ++j) red[rg * 64 + cg * 8 + j] = a[j];
  __syncthreads();
  if (t < 64) {
    float sum = 0.f;
#pragma unroll
    for (int r = 0; r < 32; ++r) sum += red[r * 64 + t];
    z[h * 512 + s * 64 + t] = sum;
  }
}

__global__ void __launch_bounds__(256) k_fin(const unsigned short* x, const float* mod, const float* z, const unsigned short* w1, const unsigned short* b1,
                                             const unsigned short* w2, const unsigned short* b2, __hip_bfloat16* out) {
  __shared__ float sred[4];
  __shared__ float x1s[512], h2s[512];
  const int t = threadIdx.x;
  float a0 = 0.f, a1 = 0.f;
#pragma unroll
  for (int h = 0; h < 32; ++h) { a0 += z[h * 512 + t]; a1 += z[h * 512 + 256 + t]; }
  const float x0 = bf2f(x[t]) + mod[1024 + t] * a0, x1 = bf2f(x[t + 256]) + mod[1024 + 256 + t] * a1;
  float mean, rstd;
  block_ln_stats(x0, x1, sred, mean, rstd);
  const float h0 = (x0 - mean) * rstd * (1.f + mod[2048 + t]) + mod[1536 + t];
  const float h1 = (x1 - mean) * rstd * (1.f + mod[2048 + 256 + t]) + mod[1536 + 256 + t];
  block_ln_stats(h0, h1, sred, mean, rstd);
  x1s[t] = x0; x1s[t + 256] = x1;
  h2s[t] = (h0 - mean) * rstd; h2s[t + 256] = (h1 - mean) * rstd;
  __syncthreads();
  const int lane = t & 63, d = blockIdx.x * 4 + (t >> 6);
  const float hv = h2s[d];
  float y = 0.f;
  for (int f = lane; f < F; f += 64) {
    const float u = fmaf(hv, bf2f(w1[f]), bf2f(b1[f]));
    const float in = 0.7978845608028654f * (u + 0.044715f * u * u * u);
    const float g = u / (1.f + __expf(-2.f * in));
    y = fmaf(g, bf2f(w2[f]), y);
  }
  y = wave_sum(y) + bf2f(b2[0]);
  if (lane == 0) out[d] = __float2bfloat16(x1s[d] + mod[2560 + d] * y);
}

extern "C" void kernel_launch(void* const* d_in, const int* in_sizes, int n_in, void* d_out, int out_size, void* d_ws, size_t ws_size, hipStream_t stream) {
  const unsigned short* x = (const unsigned short*)d_in[0];
  const unsigned short* K = (const unsigned short*)d_in[1];
  const unsigned short* V = (const unsigned short*)d_in[2];
  const unsigned short* wq = (const unsigned short*)d_in[3];
  const unsigned short* wo = (const unsigned short*)d_in[4];
  const unsigned short* w1 = (const unsigned short*)d_in[6];
  const unsigned short* b1 = (const unsigned short*)d_in[7];
  const unsigned short* w2 = (const unsigned short*)d_in[8];
  const unsigned short* b2 = (const unsigned short*)d_in[9];
  const unsigned short* c = (const unsigned short*)d_in[10];
  const unsigned short* wmod = (const unsigned short*)d_in[11];
  float* ws = (float*)d_ws;
  float* mod = ws;
  float* q = ws + 4096;
  float* pml = ws + 8192;
  float* pacc = ws + 16384;
  float* z = ws + 65536;
  k_mod<<<48, 256, 0, stream>>>(c, wmod, mod);
  k_q<<<64, 256, 0, stream>>>(x, mod, wq, q);
  k_attn<<<256, 512, 0, stream>>>(K, V, q, pml, pacc);
  k_wo<<<256, 256, 0, stream>>>(pml, pacc, wo, z);
  k_fin<<<128, 256, 0, stream>>>(x, mod, z, w1, b1, w2, b2, (__hip_bfloat16*)d_out);
}
```

```cpp
#include <hip/hip_runtime.h>
#include <hip/hip_bf16.h>
#include <stdint.h>

typedef unsigned u32;
typedef unsigned short u16;
typedef unsigned long long u64;
typedef u32 u32x4 __attribute__((ext_vector_type(4)));
typedef u32 u32x2 __attribute__((ext_vector_type(2)));
typedef float f32x4 __attribute__((ext_vector_type(4)));
typedef float f32x2 __attribute__((ext_vector_type(2)));
typedef __bf16 bf16x2 __attribute__((ext_vector_type(2)));
typedef __bf16 bf16x8 __attribute__((ext_vector_type(8)));
#define GAS __attribute__((address_space(1)))
#define CAS __attribute__((address_space(4)))
#define LAS __attribute__((address_space(3)))
typedef GAS u64 gu64;
typedef GAS u32 gu32;

#define PACE 6
#define QUIET 0
#define WONT 1
#define WOLD(p) __builtin_nontemporal_load(p)
#define NSW 6
#define LEAD (3 * NSW)
#define NLM 88
#define NLQ 80
#define NLR 100

constexpr int D = 512, H = 32, SK = 16384, DH = 128, F = 1376;
constexpr float LOG2E = 1.4426950408889634f;
constexpr float SCALE = 0.08838834764831845f;
constexpr u64 EPOCH_STEP = 0x9E3779B97F4A7C15ull;
constexpr u32 SPIN_CAP = 1u << 13;
constexpr int AUX_NT = 2, AUX_SC1 = 16;
constexpr u32 SENT = 0xFFFFFFFFu;

constexpr int POOL0 = NLM + 2 * NLQ + 5 * NLR;
static_assert(NLQ >= 48 && NLM >= 48 && NLR >= 48 && POOL0 <= 1024 && NLQ % 2 == 0 && NLM % 2 == 0 && NLR % 2 == 0, "three fixed tiles per wave, then pairs");
__device__ __forceinline__ int tile_start(int rho) {
  return rho == 0 ? 0 : rho == 1 ? NLM : rho == 2 ? NLM + NLQ : rho == 3 ? NLM + NLQ + NLR : NLM + 2 * NLQ + (rho - 3) * NLR;
}
__device__ __forceinline__ int tile_count(int rho) { return rho == 0 ? NLM : (rho == 1 || rho == 3) ? NLQ : NLR; }

constexpr int WS_E = 0;
constexpr int WS_RDY = 4096;
constexpr int WS_MG = 8192;
constexpr int WS_MS = 16384;
constexpr int WS_PC = 24576;
constexpr int WS_QG = 32768;
constexpr int WS_ML = 65536;
constexpr int WS_ZS = 69632;
constexpr int WS_AS = 98304;
constexpr int WS_END = WS_AS + 256 * 512;

__device__ __forceinline__ float asf(u32 u) { return __builtin_bit_cast(float, u); }
__device__ __forceinline__ u32 asu(float f) { return __builtin_bit_cast(u32, f); }
__device__ __forceinline__ float bflo(u32 u) { return asf(u << 16); }
__device__ __forceinline__ float bfhi(u32 u) { return asf(u & 0xffff0000u); }
__device__ __forceinline__ float bf2f(u16 b) { return asf((u32)b << 16); }
__device__ __forceinline__ float rbf(float x) { return (float)(__bf16)x; }
__device__ __forceinline__ u32 pkbf(float lo, float hi) { return __builtin_bit_cast(u32, bf16x2{(__bf16)lo, (__bf16)hi}); }
__device__ __forceinline__ float dot2(u32 k, u32 q, float acc) {
  return __builtin_amdgcn_fdot2_f32_bf16(__builtin_bit_cast(bf16x2, k), __builtin_bit_cast(bf16x2, q), acc, false);
}
template <int CTRL> __device__ __forceinline__ float dpp(float x) {
  return asf((u32)__builtin_amdgcn_mov_dpp((int)asu(x), CTRL, 0xf, 0xf, true));
}
constexpr int XOR1 = 0xB1, XOR2 = 0x4E, HMIR = 0x141, ROR8 = 0x128, ROR4 = 0x124, NBC = 0x150;
__device__ __forceinline__ float x16_max(float x) {
  auto s = __builtin_amdgcn_permlane16_swap(asu(x), asu(x), false, false);
  return fmaxf(asf(s[0]), asf(s[1]));
}
__device__ __forceinline__ float x16_sum(float x) {
  auto s = __builtin_amdgcn_permlane16_swap(asu(x), asu(x), false, false);
  return asf(s[0]) + asf(s[1]);
}
__device__ __forceinline__ float xrow_max(float x) {
  x = x16_max(x);
  auto t = __builtin_amdgcn_permlane32_swap(asu(x), asu(x), false, false);
  return fmaxf(asf(t[0]), asf(t[1]));
}
__device__ __forceinline__ float xrow_sum(float x) {
  x = x16_sum(x);
  auto t = __builtin_amdgcn_permlane32_swap(asu(x), asu(x), false, false);
  return asf(t[0]) + asf(t[1]);
}
__device__ __forceinline__ float tr16(float a, float b) {
  auto s = __builtin_amdgcn_permlane16_swap(asu(a), asu(b), false, false);
  return asf(s[0]) + asf(s[1]);
}
__device__ __forceinline__ float tr32(float a, float b) {
  auto s = __builtin_amdgcn_permlane32_swap(asu(a), asu(b), false, false);
  return asf(s[0]) + asf(s[1]);
}
__device__ __forceinline__ float rows4(float a0, float a1, float a2, float a3) { return tr32(tr16(a0, a1), tr16(a2, a3)); }
__device__ __forceinline__ float sum8(float x) { x += dpp<XOR1>(x); x += dpp<XOR2>(x); return x + dpp<HMIR>(x); }
__device__ __forceinline__ float wave_sum(float x) { x = sum8(x); x += dpp<ROR8>(x); return xrow_sum(x); }
__device__ __forceinline__ float ex2(float x) { return __builtin_amdgcn_exp2f(x); }
__device__ __forceinline__ float rcp(float x) { return __builtin_amdgcn_rcpf(x); }

__device__ __forceinline__ bool gran_ok(u32x4 g, u32 tag) { return g[1] == tag && g[3] == tag; }
__device__ __forceinline__ u32 nosent(u32 v) { return v == SENT ? 0xFFFEFFFEu : v; }
__device__ __forceinline__ void st32(gu32* p, u32 v) { __hip_atomic_store(p, nosent(v), __ATOMIC_RELAXED, __HIP_MEMORY_SCOPE_AGENT); }
__device__ __forceinline__ u32 ld32(const gu32* p) { return __hip_atomic_load(p, __ATOMIC_RELAXED, __HIP_MEMORY_SCOPE_AGENT); }
__device__ __forceinline__ bool ok4(u32x4 v) { return max(max(v[0], v[1]), max(v[2], v[3])) != SENT; }

__device__ __forceinline__ float ln_el(float x, float mb, float rden) { return rbf(rbf(x - mb) * rden); }
__device__ __forceinline__ float ln_rden(float var) { return rcp(rbf(__builtin_amdgcn_sqrtf(rbf(rbf(var) + 1e-5f)))); }
__device__ __forceinline__ f32x2 ln_part(float v) {
  const float s = wave_sum(v), d = v - s * (1.f / 64.f);
  return f32x2{s, wave_sum(d * d)};
}
__device__ __forceinline__ void ln_comb(f32x2 p, float& mb, float& rden) {
  const float mean = sum8(p[0]) * (1.f / 512.f), dv = p[0] * (1.f / 64.f) - mean;
  const float var = sum8(fmaf(dv * dv, 64.f, p[1])) * (1.f / 512.f);
  mb = rbf(mean);
  rden = ln_rden(var);
}

struct Lds {
  u32x4 tile[16][512];
  f32x4 zs[8][128];
  u32x4 ms[256];
  float red[16][128];
  u32x2 hbuf[16][8];
  f32x2 ml[16];
  f32x2 st1[8], st2[8];
  float zpart[4][16];
  f32x2 yred[16];
  float x1d[2], hd[2];
  u32 qs[64];
  u32 ctr, gcnt, zcnt, b2, qflag, rdyflag;
  u32x4 mpk[8];
};

#define TS(k) do {} while (0)

__global__ void __launch_bounds__(1024, 4)
fused(const u16* __restrict__ x, const u16* __restrict__ Kc, const u16* __restrict__ Vc, const u16* __restrict__ wq, const u16* __restrict__ wo,
      const u16* __restrict__ w1, const u16* __restrict__ b1, const u16* __restrict__ w2, const u16* __restrict__ b2, const u16* __restrict__ cc,
      const u16* __restrict__ wmod, u16* __restrict__ out, unsigned char* ws) {
  __shared__ Lds L;
  TS(0);
  const int tid = threadIdx.x, lane = tid & 63, w = __builtin_amdgcn_readfirstlane(tid >> 6);
  const int r = lane >> 4, c = lane & 15;
  const int bid = blockIdx.x, rho = bid >> 5, u = bid & 31, head = (u & 24) | ((u + rho) & 7);
  const int slot = head * 8 + rho;
  const int grp = head >> 2, cs = (u & 7) * 4 + (head & 3);
  const u64 E = *(const CAS u64*)(ws + WS_E) + EPOCH_STEP;
  const u32 tag = (u32)E;
  gu64* const RDY = (gu64*)(ws + WS_RDY);
  const __amdgpu_buffer_rsrc_t rws = __builtin_amdgcn_make_buffer_rsrc((void*)ws, 0, WS_END, 0x00020000);

  const int t0 = tile_start(rho), tend = t0 + tile_count(rho);
  const size_t kvbase = (size_t)head * SK * DH;
  const __amdgpu_buffer_rsrc_t rk = __builtin_amdgcn_make_buffer_rsrc((void*)(Kc + kvbase), 0, SK * DH * 2, 0x00020000);
  const __amdgpu_buffer_rsrc_t rv = __builtin_amdgcn_make_buffer_rsrc((void*)(Vc + kvbase), 0, SK * DH * 2, 0x00020000);
  const int voff = lane * 16;
  u32x4 kA[4], vA[4], kB[4], vB[4];
#define LDT(dst, rs, t) do { _Pragma("unroll") for (int i_ = 0; i_ < 4; ++i_) dst[i_] = __builtin_amdgcn_raw_buffer_load_b128(rs, voff + i_ * 1024, (t) * 4096, AUX_NT); } while (0)
#define PACE_WAIT() do { if (paced) { paced = !__builtin_amdgcn_readfirstlane(__hip_atomic_load(&L.qflag, __ATOMIC_RELAXED, __HIP_MEMORY_SCOPE_WORKGROUP)); \
      if (paced) asm volatile("s_waitcnt vmcnt(%0)" :: "n"(PACE - 1) : "memory"); } } while (0)
#define DMA1(rs, ldsb, so, OFS) do { PACE_WAIT(); asm volatile("s_mov_b32 m0, %0\n\ts_nop 0\n\tbuffer_load_dwordx4 %1, %2, %3 offen offset:" #OFS " nt lds" \
      :: "s"(ldsb), "v"(voff), "s"(rs), "s"(so) : "memory"); } while (0)
#define DMA4(rs, ldsb, so) do { DMA1(rs, ldsb, so, 0); DMA1(rs, ldsb, so, 1024); DMA1(rs, ldsb, so, 2048); DMA1(rs, ldsb, so, 3072); } while (0)
#define LDP(dst, rs, t) do { _Pragma("unroll") for (int i_ = 0; i_ < 4; ++i_) { PACE_WAIT(); \
    dst[i_] = __builtin_amdgcn_raw_buffer_load_b128(rs, voff + i_ * 1024, (t) * 4096, AUX_NT); asm volatile("" ::: "memory"); } } while (0)
#define WG_BARRIER() asm volatile("s_waitcnt lgkmcnt(0)\n\ts_barrier" ::: "memory")

  const bool qrole = rho == 1 || rho == 3, mrole = rho == 0 || qrole;
  const int unit = ((rho + 1) >> 1) * 32 + u;
  const int qh = rho >> 1;
  u32x4 wqv[4];
  if (qrole) {
    const __amdgpu_buffer_rsrc_t rq = __builtin_amdgcn_make_buffer_rsrc((void*)wq, 0, D * H * DH * 2, 0x00020000);
    const int qo = (32 * w + (lane >> 3)) * 8192 + head * 256 + qh * 128 + (lane & 7) * 16;
#pragma unroll
    for (int i = 0; i < 4; ++i) wqv[i] = __builtin_amdgcn_raw_buffer_load_b128(rq, qo, i * 65536, AUX_NT);
    asm volatile("" ::: "memory");
  }
  if (tid == 0) { L.ctr = t0 + 48; L.gcnt = 0; L.zcnt = 0; L.qflag = 0; L.rdyflag = 0; }
  if (w == 15) {
    const int o = lane < 32 ? WS_AS + slot * 512 + lane * 16 : lane < 36 ? WS_ZS + grp * 2048 + cs * 64 + (lane - 32) * 16
                : (lane < 40 && mrole && unit >= 32) ? WS_MS + (unit - 32) * 64 + (lane - 36) * 16 : WS_END;
    __builtin_amdgcn_raw_buffer_store_b128(u32x4{SENT, SENT, SENT, SENT}, rws, o, 0, AUX_SC1);
    __builtin_amdgcn_raw_buffer_store_b64(u32x2{lane ? 0u : SENT, lane ? 0u : SENT}, rws,
                                          lane == 0 ? WS_ML + slot * 8 : (lane == 1 && rho == 1) ? WS_PC + head * 128 : WS_END, 0, AUX_SC1);
  }
#define SUM16(p) ((((p[0] + p[1]) + (p[2] + p[3])) + ((p[4] + p[5]) + (p[6] + p[7]))) + (((p[8] + p[9]) + (p[10] + p[11])) + ((p[12] + p[13]) + (p[14] + p[15]))))
  if (mrole) {
    const __amdgpu_buffer_rsrc_t rm = __builtin_amdgcn_make_buffer_rsrc((void*)wmod, 0, D * 6 * D * 2, 0x00020000);
    const int mo = (32 * w + (lane >> 2)) * (6 * D * 2) + unit * 64 + (lane & 3) * 16;
    const u32 c0r = cc[32 * w + (lane >> 2)], c1r = cc[32 * w + 16 + (lane >> 2)];
    const u32x4 wm0 = __builtin_amdgcn_raw_buffer_load_b128(rm, mo, 0, AUX_NT), wm1 = __builtin_amdgcn_raw_buffer_load_b128(rm, mo, 16 * (6 * D * 2), AUX_NT);
    const float c0 = bflo(c0r), c1 = bflo(c1r);
    const float s0 = rbf(c0 * rcp(1.f + ex2(-c0 * LOG2E))), s1 = rbf(c1 * rcp(1.f + ex2(-c1 * LOG2E)));
    f32x2 a[4];
#pragma unroll
    for (int j = 0; j < 4; ++j) a[j] = f32x2{bflo(wm0[j]), bfhi(wm0[j])} * s0 + f32x2{bflo(wm1[j]), bfhi(wm1[j])} * s1;
    const float e0 = rows4(a[0][0], a[0][1], a[1][0], a[1][1]), e1 = rows4(a[2][0], a[2][1], a[3][0], a[3][1]);
    const bool b3 = lane & 8;
    L.red[w][(lane & 4) * 8 + 8 * (lane & 3) + ((lane & 8) >> 1) + r] = (b3 ? e1 : e0) + dpp<ROR8>(b3 ? e0 : e1);
    WG_BARRIER();
    if (w == 15) {
      float p[16];
#pragma unroll
      for (int i = 0; i < 16; ++i) p[i] = L.red[i][lane];
      float v = SUM16(p);
      auto sw = __builtin_amdgcn_permlane32_swap(asu(v), asu(v), false, false);
      v = asf(sw[0]) + asf(sw[1]);
      const float v1 = dpp<XOR1>(v);
      const u32 pr = pkbf(v, v1);
      const u32 pr2 = asu(dpp<XOR2>(asf(pr)));
      if (unit < 32) {
        __builtin_amdgcn_raw_buffer_store_b128(u32x4{pr, tag, pr2, tag}, rws, (lane < 32 && !(lane & 3)) ? WS_MG + unit * 128 + lane * 4 : WS_END, 0, AUX_SC1);
      } else {
        __builtin_amdgcn_raw_buffer_store_b64(u32x2{nosent(pr), nosent(pr2)}, rws, (lane < 32 && !(lane & 3)) ? WS_MS + (unit - 32) * 64 + lane * 2 : WS_END, 0, AUX_SC1);
      }
    }
    TS(1);
  }
  WG_BARRIER();
  if (qrole) {
    const u32x4 xa = *(const u32x4*)(x + lane * 8);
    const u32x2 xme = *(const u32x2*)(x + 32 * w + 4 * (lane & 7));
    float xv[8];
#pragma unroll
    for (int i = 0; i < 4; ++i) { xv[2 * i] = bflo(xa[i]); xv[2 * i + 1] = bfhi(xa[i]); }
    const float mean = wave_sum(((xv[0] + xv[1]) + (xv[2] + xv[3])) + ((xv[4] + xv[5]) + (xv[6] + xv[7]))) * (1.f / 512.f);
    float qq = 0.f;
#pragma unroll
    for (int i = 0; i < 8; ++i) { const float d = xv[i] - mean; qq = fmaf(d, d, qq); }
    const float mb = rbf(mean), rden = ln_rden(wave_sum(qq) * (1.f / 512.f));
    float xn[4] = {ln_el(bflo(xme[0]), mb, rden), ln_el(bfhi(xme[0]), mb, rden), ln_el(bflo(xme[1]), mb, rden), ln_el(bfhi(xme[1]), mb, rden)};
    u32 wp[2][8];
#pragma unroll
    for (int i = 0; i < 2; ++i)
#pragma unroll
      for (int j = 0; j < 4; ++j) {
        wp[i][2 * j] = __builtin_amdgcn_perm(wqv[2 * i + 1][j], wqv[2 * i][j], 0x05040100u);
        wp[i][2 * j + 1] = __builtin_amdgcn_perm(wqv[2 * i + 1][j], wqv[2 * i][j], 0x07060302u);
      }
#pragma unroll
    for (int i = 0; i < 2; ++i)
      asm volatile("" : "+v"(wp[i][0]), "+v"(wp[i][1]), "+v"(wp[i][2]), "+v"(wp[i][3]), "+v"(wp[i][4]), "+v"(wp[i][5]), "+v"(wp[i][6]), "+v"(wp[i][7]));
    asm volatile("" : "+v"(xn[0]), "+v"(xn[1]), "+v"(xn[2]), "+v"(xn[3]));
    const int go = lane < 16 ? WS_MG + (lane >> 3) * 2048 + w * 128 + (lane & 7) * 16 : WS_END;
    u32x4 g;
    _Pragma("nounroll") for (u32 spin = 0; spin < SPIN_CAP; ++spin) {
      g = __builtin_amdgcn_raw_buffer_load_b128(rws, go, 0, AUX_SC1);
      if ((__ballot(gran_ok(g, tag)) & 0xffffull) == 0xffffull) break;
      __builtin_amdgcn_s_sleep(3);
    }
    TS(1);
    const u32 ca0 = asu(dpp<ROR8>(asf(g[0]))), ca1 = asu(dpp<ROR8>(asf(g[2])));
    const float h0 = rbf(xn[0] * rbf(1.f + bflo(ca0))) + bflo(g[0]), h1 = rbf(xn[1] * rbf(1.f + bfhi(ca0))) + bfhi(g[0]);
    const float h2 = rbf(xn[2] * rbf(1.f + bflo(ca1))) + bflo(g[2]), h3 = rbf(xn[3] * rbf(1.f + bfhi(ca1))) + bfhi(g[2]);
    if (lane < 8) {
      u16* hb = (u16*)&L.hbuf[w][4 * (lane & 1)] + (lane >> 1);
      const u32 p01 = pkbf(h0, h1), p23 = pkbf(h2, h3);
      hb[0] = (u16)p01; hb[4] = (u16)(p01 >> 16); hb[8] = (u16)p23; hb[12] = (u16)(p23 >> 16);
    }
    asm volatile("s_waitcnt lgkmcnt(0)" ::: "memory");
    const u32x2 hp = L.hbuf[w][lane >> 3];
    float a[8];
#pragma unroll
    for (int j = 0; j < 8; ++j) a[j] = dot2(wp[1][j], hp[1], dot2(wp[0][j], hp[0], 0.f));
    const float e0 = rows4(a[0], a[1], a[2], a[3]), e1 = rows4(a[4], a[5], a[6], a[7]);
    const bool b3 = lane & 8;
    L.red[w][8 * (lane & 7) + ((lane & 8) >> 1) + r] = (b3 ? e1 : e0) + dpp<ROR8>(b3 ? e0 : e1);
    WG_BARRIER();
    if (w == 15) {
      float p[16];
#pragma unroll
      for (int i = 0; i < 16; ++i) p[i] = L.red[i][lane];
      const float v = SUM16(p);
      const u32 pr = pkbf(v, dpp<XOR1>(v)), pr2 = asu(dpp<XOR2>(asf(pr)));
      __builtin_amdgcn_raw_buffer_store_b128(u32x4{pr, tag, pr2, tag}, rws, (lane & 3) ? WS_END : WS_QG + head * 512 + qh * 256 + lane * 4, 0, AUX_SC1);
    }
    TS(2);
    WG_BARRIER();
  }
  if (w == 0) {
    const int go = lane < 32 ? WS_QG + head * 512 + lane * 16 : WS_END;
    u32x4 g = __builtin_amdgcn_raw_buffer_load_b128(rws, go, 0, AUX_SC1);
    __builtin_amdgcn_s_sleep(8);
    _Pragma("nounroll") for (u32 spin = 0; spin < SPIN_CAP; ++spin) {
      const u32x4 g2 = __builtin_amdgcn_raw_buffer_load_b128(rws, go, 0, AUX_SC1);
      asm volatile("" ::: "memory");
      if ((u32)__ballot(gran_ok(g, tag)) == 0xffffffffu) { if (lane < 32) *(u32x2*)&L.qs[2 * lane] = u32x2{g[0], g[2]}; break; }
      __builtin_amdgcn_s_sleep(2);
      g = __builtin_amdgcn_raw_buffer_load_b128(rws, go, 0, AUX_SC1);
      asm volatile("" ::: "memory");
      if ((u32)__ballot(gran_ok(g2, tag)) == 0xffffffffu) { if (lane < 32) *(u32x2*)&L.qs[2 * lane] = u32x2{g2[0], g2[2]}; break; }
      __builtin_amdgcn_s_sleep(2);
    }
    asm volatile("s_waitcnt lgkmcnt(0)" ::: "memory");
    if (lane == 0) L.qflag = 1;
    TS(3);
  }
  bool paced = w != 0;
  const u32 tl_addr = (u32)(size_t)(LAS u32x4*)&L.tile[w][0];
  DMA4(rk, tl_addr, (t0 + w) * 4096);
  if (w == 15) {
    asm volatile("s_waitcnt vmcnt(4)" ::: "memory");
    if (lane == 0) __hip_atomic_store(RDY + slot, E, __ATOMIC_RELAXED, __HIP_MEMORY_SCOPE_AGENT);
  }
  DMA4(rv, tl_addr + 4096, (t0 + w) * 4096);
  const u32x4 wov = WOLD((const u32x4*)(wo + (size_t)(grp * 512 + 32 * w + (lane >> 1)) * D + 16 * cs + 8 * (lane & 1)));
  const u16* ap_ = tid < 172 ? w1 + tid * 8 : tid < 344 ? b1 + (tid - 172) * 8 : tid < 516 ? w2 + (tid - 344) * 8 : x + (tid < 580 ? tid - 516 : 0) * 8;
  const u32x4 auxv = *(const u32x4*)ap_;
  const u32 b2raw = __builtin_amdgcn_raw_buffer_load_b16(__builtin_amdgcn_make_buffer_rsrc((void*)b2, 0, 2, 0x00020000), 0, 0, 0);
  u32x4 rd0, rd1;
#define SAMPLE_RDY() do { rd0 = __builtin_amdgcn_raw_buffer_load_b128(rws, w == 4 ? WS_RDY + lane * 16 : WS_END, 0, AUX_SC1); \
    rd1 = __builtin_amdgcn_raw_buffer_load_b128(rws, w == 4 ? WS_RDY + 1024 + lane * 16 : WS_END, 0, AUX_SC1); } while (0)
#define RDY_OK() __all(((rd0[0] ^ elo) | (rd0[2] ^ elo) | (rd1[0] ^ elo) | (rd1[2] ^ elo) | (rd0[1] ^ ehi) | (rd0[3] ^ ehi) | (rd1[1] ^ ehi) | (rd1[3] ^ ehi)) == 0u)
  const u32 elo = (u32)E, ehi = (u32)(E >> 32);
  SAMPLE_RDY();
  asm volatile("" ::: "memory");
  LDP(kA, rk, t0 + 16 + w); LDP(vA, rv, t0 + 16 + w); LDP(kB, rk, t0 + 32 + w); LDP(vB, rv, t0 + 32 + w);
  WG_BARRIER();
  if (w != 0) TS(3);
  const u32x4 qp = *(const u32x4*)&L.qs[4 * c];

  float m = -INFINITY, l = 0.f;
  f32x2 acc[4] = {{0.f, 0.f}, {0.f, 0.f}, {0.f, 0.f}, {0.f, 0.f}};
  const bool c8 = c & 8, c4 = c & 4;
  const u32 ctr_addr = (u32)(size_t)(LAS u32*)&L.ctr, two = 2u;
#define GRAB(dst) do { u64 sv_; asm volatile("s_mov_b64 %1, exec\n\ts_mov_b64 exec, 1\n\tds_add_rtn_u32 %0, %2, %3\n\ts_mov_b64 exec, %1" \
    : "=&v"(dst), "=&s"(sv_) : "v"(ctr_addr), "v"(two) : "memory"); } while (0)
#define GRAB_WAIT(dst) asm volatile("s_waitcnt lgkmcnt(0)" : "+v"(dst) :: "memory")
#define TICKET(want) (u32)__builtin_amdgcn_raw_ptr_buffer_atomic_add_i32(2, rws, ((want) && lane == 0) ? WS_PC + head * 128 : WS_END, 0, 0)
#define PIN() do { asm volatile("" ::: "memory"); __builtin_amdgcn_sched_barrier(0); } while (0)
#define QK(xx, kk) do { \
    _Pragma("unroll") for (int t_ = 0; t_ < 4; ++t_) { float d_ = dot2(kk[t_][0], qp[0], 0.f); d_ = dot2(kk[t_][1], qp[1], d_); d_ = dot2(kk[t_][2], qp[2], d_); xx[t_] = dot2(kk[t_][3], qp[3], d_); } \
    asm volatile("" : "+v"(xx[0]), "+v"(xx[1]), "+v"(xx[2]), "+v"(xx[3])); } while (0)
#define SMPV(x_, vv) do { \
    const float y0_ = (c8 ? x_[2] : x_[0]) + dpp<ROR8>(c8 ? x_[0] : x_[2]); \
    const float y1_ = (c8 ? x_[3] : x_[1]) + dpp<ROR8>(c8 ? x_[1] : x_[3]); \
    float z_ = (c4 ? y1_ : y0_) + dpp<HMIR>(c4 ? y0_ : y1_); \
    z_ += dpp<XOR2>(z_); z_ += dpp<XOR1>(z_); \
    const float s_ = rbf(rbf(z_) * SCALE); \
    float mx_ = fmaxf(s_, dpp<HMIR>(s_)); mx_ = fmaxf(mx_, dpp<ROR8>(mx_)); \
    const float mn_ = fmaxf(m, mx_); \
    const float al_ = ex2((m - mn_) * LOG2E), p_ = ex2((s_ - mn_) * LOG2E); \
    m = mn_; l = fmaf(l, al_, p_); \
    _Pragma("unroll") for (int j_ = 0; j_ < 4; ++j_) acc[j_] *= al_; \
    { const float p0_ = dpp<NBC + 0>(p_), p1_ = dpp<NBC + 4>(p_), p2_ = dpp<NBC + 8>(p_), p3_ = dpp<NBC + 12>(p_); \
      _Pragma("unroll") for (int j_ = 0; j_ < 4; ++j_) { \
        acc[j_] += f32x2{bflo(vv[0][j_]), bfhi(vv[0][j_])} * p0_; acc[j_] += f32x2{bflo(vv[1][j_]), bfhi(vv[1][j_])} * p1_; \
        acc[j_] += f32x2{bflo(vv[2][j_]), bfhi(vv[2][j_])} * p2_; acc[j_] += f32x2{bflo(vv[3][j_]), bfhi(vv[3][j_])} * p3_; } } \
    asm volatile("" : "+v"(acc[0]), "+v"(acc[1]), "+v"(acc[2]), "+v"(acc[3]), "+v"(l), "+v"(m)); } while (0)
  float xa_[4], xb_[4];
  QK(xa_, kA); PIN();
#pragma unroll
  for (int i = 0; i < 4; ++i) kA[i] = L.tile[w][i * 64 + lane];
  PIN();
  SMPV(xa_, vA); PIN();
#pragma unroll
  for (int i = 0; i < 4; ++i) vA[i] = L.tile[w][256 + i * 64 + lane];
  PIN();
  {
    u16* wf = (u16*)&L.tile[w][(lane >> 4) * 16 + (lane & 1) * 8] + ((lane >> 1) & 7);
#pragma unroll
    for (int j = 0; j < 4; ++j) { wf[16 * j] = (u16)(wov[j] & 0xffffu); wf[16 * j + 8] = (u16)(wov[j] >> 16); }
    L.tile[w][64 + lane] = auxv;
    if (tid == 0) L.b2 = b2raw;
    if (RDY_OK() && lane == 0) L.rdyflag = 1;
    PIN();
  }
  TS(11);
  if (w < NSW) {
    bool have = false, pending = false;
    u32 tk = 0, tkval = 0;
    for (int it = 0; it < 1024; ++it) {
      int tv;
      GRAB(tv);
      QK(xa_, kA); QK(xb_, kB);
      __builtin_amdgcn_sched_barrier(0);
      GRAB_WAIT(tv);
      int tn = __builtin_amdgcn_readfirstlane(tv);
      if (pending) { tkval = __builtin_amdgcn_readfirstlane(tk); have = true; }
      if (tn < tend) pending = !have && tn + LEAD >= tend;
      else {
        if (!have) tkval = __builtin_amdgcn_readfirstlane(TICKET(true));
        tn = min(POOL0 + (int)tkval, 1024); have = false; pending = tn < 1024;
      }
      LDT(kA, rk, tn); LDT(kB, rk, tn + 1); tk = TICKET(pending);
      PIN();
      SMPV(xa_, vA); PIN();
      LDT(vA, rv, tn); PIN();
      SMPV(xb_, vB); PIN();
      LDT(vB, rv, tn + 1); PIN();
      if (tn >= 1024) break;
    }
  } else {
    QK(xa_, kA); QK(xb_, kB); PIN();
    SMPV(xa_, vA); PIN();
    SMPV(xb_, vB); PIN();
  }
  TS(4);

  {
    float lr = l + dpp<HMIR>(l); lr += dpp<ROR8>(lr);
    const float Mw = xrow_max(m), sc = ex2((m - Mw) * LOG2E);
    lr = xrow_sum(lr * sc);
#pragma unroll
    for (int j = 0; j < 4; ++j) acc[j] *= sc;
    L.red[w][8 * c + r] = rows4(acc[0][0], acc[0][1], acc[1][0], acc[1][1]);
    L.red[w][8 * c + 4 + r] = rows4(acc[2][0], acc[2][1], acc[3][0], acc[3][1]);
    if (lane == 0) L.ml[w] = f32x2{Mw, lr};
  }
  WG_BARRIER();
  TS(5);
#define LDS_WAIT(word, n) do { _Pragma("nounroll") for (u32 spin_ = 0; spin_ < (1u << 22); ++spin_) \
    if (__hip_atomic_load(&(word), __ATOMIC_RELAXED, __HIP_MEMORY_SCOPE_WORKGROUP) >= (n)) break; \
    asm volatile("" ::: "memory"); } while (0)
#define LDS_SIGNAL(word) do { asm volatile("s_waitcnt lgkmcnt(0)" ::: "memory"); \
    if (lane == 0) __hip_atomic_fetch_add(&(word), 1u, __ATOMIC_RELAXED, __HIP_MEMORY_SCOPE_WORKGROUP); } while (0)
  const int dd = (tid - 512) & 511;
  if (w < 4) {
    if (w == 0) {
      const f32x2 mlv = L.ml[c];
      f32x2 pv[16];
#pragma unroll
      for (int i = 0; i < 16; ++i) pv[i] = *(const f32x2*)&L.red[i][2 * lane];
      float M = fmaxf(mlv[0], dpp<XOR1>(mlv[0])); M = fmaxf(M, dpp<XOR2>(M)); M = fmaxf(M, dpp<HMIR>(M)); M = fmaxf(M, dpp<ROR8>(M));
      const float wt = ex2((mlv[0] - M) * LOG2E);
      float ls = sum8(mlv[1] * wt); ls += dpp<ROR8>(ls);
      f32x2 o0 = pv[0] * dpp<NBC + 0>(wt), o1 = pv[1] * dpp<NBC + 1>(wt), o2 = pv[2] * dpp<NBC + 2>(wt), o3 = pv[3] * dpp<NBC + 3>(wt);
#define MRG(i) o0 += pv[i] * dpp<NBC + i>(wt); o1 += pv[i + 1] * dpp<NBC + i + 1>(wt); o2 += pv[i + 2] * dpp<NBC + i + 2>(wt); o3 += pv[i + 3] * dpp<NBC + i + 3>(wt);
      MRG(4) MRG(8) MRG(12)
      const f32x2 o = (o0 + o1) + (o2 + o3);
      const u32 hi = nosent(pkbf(o[0], o[1])), lo = nosent(pkbf(o[0] - bflo(hi), o[1] - bfhi(hi)));
      const float sel = asf((lane & 1) ? hi : lo), own = asf((lane & 1) ? lo : hi);
      const float nb = dpp<XOR1>(sel);
      const float e0 = (lane & 1) ? nb : own, e1 = (lane & 1) ? own : nb;
      const u32x4 pc = {asu(e0), asu(e1), asu(dpp<XOR2>(e0)), asu(dpp<XOR2>(e1))};
      __builtin_amdgcn_raw_buffer_store_b128(pc, rws, (lane & 2) ? WS_END : WS_AS + slot * 512 + (lane & 1) * 256 + (lane >> 2) * 16, 0, AUX_SC1);
      __builtin_amdgcn_raw_buffer_store_b64(u32x2{nosent(asu(M)), nosent(asu(ls))}, rws, lane == 0 ? WS_ML + slot * 8 : WS_END, 0, AUX_SC1);
      TS(10);
    }
    u32x4 bfr[4];
#pragma unroll
    for (int ks = 0; ks < 4; ++ks) bfr[ks] = L.tile[4 * w + ks][lane];
    LDS_WAIT(L.rdyflag, 1u);
    const int ps = (grp * 4 + w) * 8 + (lane & 7);
    const int ao = WS_AS + ps * 512 + (lane & 8) * 32 + r * 16;
    u32x4 af[4];
    u32x2 mlp;
    bool ok = false;
    _Pragma("nounroll") for (u32 spin = 0; spin < SPIN_CAP; ++spin) {
      if (!ok) {
#pragma unroll
        for (int ks = 0; ks < 4; ++ks) af[ks] = __builtin_amdgcn_raw_buffer_load_b128(rws, ao + 64 * ks, 0, AUX_SC1);
        mlp = __builtin_amdgcn_raw_buffer_load_b64(rws, WS_ML + ps * 8, 0, AUX_SC1);
        ok = ok4(af[0]) && ok4(af[1]) && ok4(af[2]) && ok4(af[3]) && max(mlp[0], mlp[1]) != SENT;
      }
      const u64 miss = __ballot(!ok);
      if (!miss) break;
      if (__builtin_popcountll(miss) > 16) __builtin_amdgcn_s_sleep(24); else __builtin_amdgcn_s_sleep(2);
    }
    TS(6);
    f32x4 d4 = {0.f, 0.f, 0.f, 0.f};
#pragma unroll
    for (int ks = 0; ks < 4; ++ks) d4 = __builtin_amdgcn_mfma_f32_16x16x32_bf16(__builtin_bit_cast(bf16x8, af[ks]), __builtin_bit_cast(bf16x8, bfr[ks]), d4, 0, 0, 0);
    const float Ms = asf(mlp[0]);
    float Mx = fmaxf(Ms, dpp<XOR1>(Ms)); Mx = fmaxf(Mx, dpp<XOR2>(Mx)); Mx = fmaxf(Mx, dpp<HMIR>(Mx));
    const float wt = ex2((Ms - Mx) * LOG2E);
    const float cf = wt * rcp(sum8(wt * asf(mlp[1])));
    const float cs4 = (r & 1) ? dpp<HMIR>(cf) : cf;
    const bool od = r & 1;
    const float zp = xrow_sum(fmaf(od ? dpp<NBC + 3>(cs4) : dpp<NBC + 0>(cs4), d4[0], (od ? dpp<NBC + 2>(cs4) : dpp<NBC + 1>(cs4)) * d4[1]) +
                              fmaf(od ? dpp<NBC + 1>(cs4) : dpp<NBC + 2>(cs4), d4[2], (od ? dpp<NBC + 0>(cs4) : dpp<NBC + 3>(cs4)) * d4[3]));
    if (lane < 16) L.zpart[w][lane] = zp;
    LDS_SIGNAL(L.zcnt);
    if (w == 0) {
      LDS_WAIT(L.zcnt, 4u);
      const f32x4 z = (*(const f32x4*)&L.zpart[0][4 * (lane & 3)] + *(const f32x4*)&L.zpart[1][4 * (lane & 3)]) +
                      (*(const f32x4*)&L.zpart[2][4 * (lane & 3)] + *(const f32x4*)&L.zpart[3][4 * (lane & 3)]);
      const u32x4 zz = {nosent(asu(z[0])), nosent(asu(z[1])), nosent(asu(z[2])), nosent(asu(z[3]))};
      __builtin_amdgcn_raw_buffer_store_b128(zz, rws, lane < 4 ? WS_ZS + grp * 2048 + cs * 64 + lane * 16 : WS_END, 0, AUX_SC1);
    }
    TS(7);
  } else if (w == 4) {
    if (!__builtin_amdgcn_readfirstlane(L.rdyflag)) {
      _Pragma("nounroll") for (u32 spin = 0; spin < SPIN_CAP; ++spin) { SAMPLE_RDY(); if (RDY_OK()) break; }
      if (lane == 0) L.rdyflag = 1;
    }
    u32x4 mv[4];
    _Pragma("nounroll") for (u32 spin = 0; spin < SPIN_CAP; ++spin) {
#pragma unroll
      for (int i = 0; i < 4; ++i) mv[i] = __builtin_amdgcn_raw_buffer_load_b128(rws, WS_MS + i * 1024 + lane * 16, 0, AUX_SC1);
      if (__all(ok4(mv[0]) && ok4(mv[1]) && ok4(mv[2]) && ok4(mv[3]))) break;
      __builtin_amdgcn_s_sleep(8);
    }
#pragma unroll
    for (int i = 0; i < 4; ++i) L.ms[i * 64 + lane] = mv[i];
    LDS_SIGNAL(L.gcnt);
  }
#define AUX32(i) (((const u32*)&L.tile[(i) >> 8][64])[(i) & 255])
#define AUX16(i) (((const u16*)&L.tile[(i) >> 9][64])[(i) & 511])
  float x1 = 0.f, xsb = 0.f, xcb = 0.f;
  if (w >= 8) {
    LDS_WAIT(L.rdyflag, 1u);
    const int zo = WS_ZS + (w - 8) * 2048 + lane * 16;
    u32x4 z0, z1;
    bool ok0 = false, ok1 = false;
    _Pragma("nounroll") for (u32 spin = 0; spin < SPIN_CAP; ++spin) {
      if (!ok0) z0 = __builtin_amdgcn_raw_buffer_load_b128(rws, zo, 0, AUX_SC1);
      if (!ok1) z1 = __builtin_amdgcn_raw_buffer_load_b128(rws, zo + 1024, 0, AUX_SC1);
      ok0 = ok4(z0); ok1 = ok4(z1);
      const u64 miss = __ballot(!(ok0 && ok1));
      if (!miss) break;
      if (__builtin_popcountll(miss) > 48) __builtin_amdgcn_s_sleep(16); else __builtin_amdgcn_s_sleep(2);
    }
    L.zs[w - 8][lane] = __builtin_bit_cast(f32x4, z0);
    L.zs[w - 8][64 + lane] = __builtin_bit_cast(f32x4, z1);
    LDS_SIGNAL(L.gcnt);
    const float xv = bf2f(AUX16(3 * F + dd));
    LDS_WAIT(L.gcnt, 9u);
    TS(6);
    const float* zf = (const float*)L.zs + dd;
    const u16* msv = (const u16*)L.ms + dd;
    const float at = ((zf[0] + zf[512]) + (zf[1024] + zf[1536])) + ((zf[2048] + zf[2560]) + (zf[3072] + zf[3584]));
    x1 = rbf(xv + rbf(bf2f(msv[0]) * rbf(at)));
    xsb = bf2f(msv[512]); xcb = rbf(1.f + bf2f(msv[1024]));
    const f32x2 p = ln_part(x1);
    if (lane == 0) L.st1[w - 8] = p;
  }
  const int fp = min(tid, F / 2 - 1);
  const u32 pw1 = AUX32(fp), pb1 = AUX32(F / 2 + fp), pw2 = tid < F / 2 ? AUX32(F + fp) : 0u;
  WG_BARRIER();
  TS(8);
  if (w >= 8) {
    float mb, rden;
    ln_comb(L.st1[lane & 7], mb, rden);
    const float hh = rbf(rbf(ln_el(x1, mb, rden) * xcb) + xsb);
    if ((dd >> 1) == bid) { L.x1d[dd & 1] = x1; L.hd[dd & 1] = hh; }
    const f32x2 p = ln_part(hh);
    if (lane == 0) L.st2[w - 8] = p;
  }
  WG_BARRIER();
  TS(9);
  if (w < 11) {
    float mb, rden;
    ln_comb(L.st2[lane & 7], mb, rden);
    const f32x2 w1f = {bflo(pw1), bfhi(pw1)}, b1f = {bflo(pb1), bfhi(pb1)};
    f32x2 y;
#pragma unroll
    for (int k = 0; k < 2; ++k) {
      const float t = ln_el(L.hd[k], mb, rden);
      const f32x2 tw = w1f * t;
      const u32 twb = pkbf(tw[0], tw[1]);
      const f32x2 us = f32x2{bflo(twb), bfhi(twb)} + b1f;
      const u32 ub = pkbf(us[0], us[1]);
      const f32x2 uu = {bflo(ub), bfhi(ub)};
      constexpr float KK = -2.f * 0.7978845608028654f * LOG2E;
      const f32x2 zz = uu * (uu * uu * (0.044715f * KK) + KK);
      const f32x2 sg = {rcp(1.f + ex2(zz[0])), rcp(1.f + ex2(zz[1]))};
      const f32x2 gg = uu * sg;
      y[k] = wave_sum(dot2(pkbf(gg[0], gg[1]), pw2, 0.f));
    }
    if (lane == 0) L.yred[w] = y;
  }
  WG_BARRIER();
  if (w == 0) {
    const int k = r & 1;
    float y = c < 11 ? L.yred[c][k] : 0.f;
    y = sum8(y); y += dpp<ROR8>(y);
    if (lane == 0 || lane == 16) {
      const float yy = rbf(rbf(y) + asf(L.b2 << 16));
      out[2 * bid + k] = (u16)(pkbf(L.x1d[k] + rbf(bf2f(((const u16*)L.ms)[1536 + 2 * bid + k]) * yy), 0.f) & 0xffffu);
    }
    if (bid == 0 && lane == 0) __hip_atomic_store((gu64*)(ws + WS_E), E, __ATOMIC_RELAXED, __HIP_MEMORY_SCOPE_AGENT);
  }
}

extern "C" void kernel_launch(void* const* d_in, const int* in_sizes, int n_in, void* d_out, int out_size, void* d_ws, size_t ws_size, hipStream_t stream) {
  fused<<<256, 1024, 0, stream>>>((const u16*)d_in[0], (const u16*)d_in[1], (const u16*)d_in[2], (const u16*)d_in[3], (const u16*)d_in[4],
                                  (const u16*)d_in[6], (const u16*)d_in[7], (const u16*)d_in[8], (const u16*)d_in[9], (const u16*)d_in[10],
                                  (const u16*)d_in[11], (u16*)d_out, (unsigned char*)d_ws);
}
```

```cpp
#include <hip/hip_runtime.h>
#include <hip/hip_bf16.h>
#include <stdint.h>

typedef unsigned u32;
typedef unsigned short u16;
typedef unsigned long long u64;
typedef u32 u32x4 __attribute__((ext_vector_type(4)));
typedef u32 u32x2 __attribute__((ext_vector_type(2)));
typedef float f32x4 __attribute__((ext_vector_type(4)));
typedef float f32x2 __attribute__((ext_vector_type(2)));
typedef __bf16 bf16x2 __attribute__((ext_vector_type(2)));
typedef __bf16 bf16x8 __attribute__((ext_vector_type(8)));
#define GAS __attribute__((address_space(1)))
#define CAS __attribute__((address_space(4)))
#define LAS __attribute__((address_space(3)))
typedef GAS u64 gu64;
typedef GAS u32 gu32;

#define PACE 6
#define QUIET 0
#define WONT 1
#define WOLD(p) __builtin_nontemporal_load(p)
#define NT0 126
#define NT1 116
#define NT2 127

constexpr int D = 512, H = 32, SK = 16384, DH = 128, F = 1376;
constexpr float LOG2E = 1.4426950408889634f;
constexpr float SCALE = 0.08838834764831845f;
constexpr u64 EPOCH_STEP = 0x9E3779B97F4A7C15ull;
constexpr u32 SPIN_CAP = 1u << 13;
constexpr int AUX_NT = 2, AUX_SC1 = 16;
constexpr u32 SENT = 0xFFFFFFFFu;

constexpr int NTR = (1024 - NT0 - NT1 - NT2) / 5, NTL = 1024 - NT0 - NT1 - NT2 - 4 * NTR;
__device__ __forceinline__ int tile_start(int rho) {
  return rho == 0 ? 0 : rho == 1 ? NT0 : rho == 2 ? NT0 + NT1 : NT0 + NT1 + NT2 + (rho - 3) * NTR;
}
__device__ __forceinline__ int tile_count(int rho) { return rho == 0 ? NT0 : rho == 1 ? NT1 : rho == 2 ? NT2 : rho == 7 ? NTL : NTR; }

constexpr int WS_E = 0;
constexpr int WS_RDY = 4096;
constexpr int WS_MG = 8192;
constexpr int WS_MS = 16384;
constexpr int WS_QG = 32768;
constexpr int WS_ML = 65536;
constexpr int WS_ZS = 69632;
constexpr int WS_AS = 98304;
constexpr int WS_END = WS_AS + 256 * 512;

__device__ __forceinline__ float asf(u32 u) { return __builtin_bit_cast(float, u); }
__device__ __forceinline__ u32 asu(float f) { return __builtin_bit_cast(u32, f); }
__device__ __forceinline__ float bflo(u32 u) { return asf(u << 16); }
__device__ __forceinline__ float bfhi(u32 u) { return asf(u & 0xffff0000u); }
__device__ __forceinline__ float bf2f(u16 b) { return asf((u32)b << 16); }
__device__ __forceinline__ float rbf(float x) { return (float)(__bf16)x; }
__device__ __forceinline__ u32 pkbf(float lo, float hi) { return __builtin_bit_cast(u32, bf16x2{(__bf16)lo, (__bf16)hi}); }
__device__ __forceinline__ float dot2(u32 k, u32 q, float acc) {
  return __builtin_amdgcn_fdot2_f32_bf16(__builtin_bit_cast(bf16x2, k), __builtin_bit_cast(bf16x2, q), acc, false);
}
template <int CTRL> __device__ __forceinline__ float dpp(float x) {
  return asf((u32)__builtin_amdgcn_mov_dpp((int)asu(x), CTRL, 0xf, 0xf, true));
}
constexpr int XOR1 = 0xB1, XOR2 = 0x4E, HMIR = 0x141, ROR8 = 0x128, ROR4 = 0x124, NBC = 0x150;
__device__ __forceinline__ float x16_max(float x) {
  auto s = __builtin_amdgcn_permlane16_swap(asu(x), asu(x), false, false);
  return fmaxf(asf(s[0]), asf(s[1]));
}
__device__ __forceinline__ float x16_sum(float x) {
  auto s = __builtin_amdgcn_permlane16_swap(asu(x), asu(x), false, false);
  return asf(s[0]) + asf(s[1]);
}
__device__ __forceinline__ float xrow_max(float x) {
  x = x16_max(x);
  auto t = __builtin_amdgcn_permlane32_swap(asu(x), asu(x), false, false);
  return fmaxf(asf(t[0]), asf(t[1]));
}
__device__ __forceinline__ float xrow_sum(float x) {
  x = x16_sum(x);
  auto t = __builtin_amdgcn_permlane32_swap(asu(x), asu(x), false, false);
  return asf(t[0]) + asf(t[1]);
}
__device__ __forceinline__ float tr16(float a, float b) {
  auto s = __builtin_amdgcn_permlane16_swap(asu(a), asu(b), false, false);
  return asf(s[0]) + asf(s[1]);
}
__device__ __forceinline__ float tr32(float a, float b) {
  auto s = __builtin_amdgcn_permlane32_swap(asu(a), asu(b), false, false);
  return asf(s[0]) + asf(s[1]);
}
__device__ __forceinline__ float rows4(float a0, float a1, float a2, float a3) { return tr32(tr16(a0, a1), tr16(a2, a3)); }
__device__ __forceinline__ float sum8(float x) { x += dpp<XOR1>(x); x += dpp<XOR2>(x); return x + dpp<HMIR>(x); }
__device__ __forceinline__ float wave_sum(float x) { x = sum8(x); x += dpp<ROR8>(x); return xrow_sum(x); }
__device__ __forceinline__ float ex2(float x) { return __builtin_amdgcn_exp2f(x); }
__device__ __forceinline__ float rcp(float x) { return __builtin_amdgcn_rcpf(x); }

__device__ __forceinline__ u32 r24(float v) { const u32 b = asu(v); return (v != v) ? 0x7FC00000u : ((b + 0x80u) & 0xFFFFFF00u); }
__device__ __forceinline__ void st_gran(gu64* p, float v, u32 tlo, u32 thi) {
  const u64 g = ((u64)thi << 32) | (u64)(tlo | (r24(v) >> 8));
  __hip_atomic_store(p, g, __ATOMIC_RELAXED, __HIP_MEMORY_SCOPE_AGENT);
}
__device__ __forceinline__ u64 ld_gran(const gu64* p) { return __hip_atomic_load(p, __ATOMIC_RELAXED, __HIP_MEMORY_SCOPE_AGENT); }
__device__ __forceinline__ bool gran_ok(u64 g, u32 tlo, u32 thi) { return (u32)(g >> 32) == thi && (((u32)g ^ tlo) < 0x01000000u); }
__device__ __forceinline__ float gran_val(u64 g) { return asf((u32)g << 8); }
__device__ __forceinline__ u32 nosent(u32 v) { return v == SENT ? 0xFFFEFFFEu : v; }
__device__ __forceinline__ void st32(gu32* p, u32 v) { __hip_atomic_store(p, nosent(v), __ATOMIC_RELAXED, __HIP_MEMORY_SCOPE_AGENT); }
__device__ __forceinline__ u32 ld32(const gu32* p) { return __hip_atomic_load(p, __ATOMIC_RELAXED, __HIP_MEMORY_SCOPE_AGENT); }
__device__ __forceinline__ bool ok4(u32x4 v) { return max(max(v[0], v[1]), max(v[2], v[3])) != SENT; }

__device__ __forceinline__ float ln_el(float x, float mb, float rden) { return rbf(rbf(x - mb) * rden); }
__device__ __forceinline__ float ln_rden(float var) { return rcp(rbf(__builtin_amdgcn_sqrtf(rbf(rbf(var) + 1e-5f)))); }
__device__ __forceinline__ f32x2 ln_part(float v) {
  const float s = wave_sum(v), d = v - s * (1.f / 64.f);
  return f32x2{s, wave_sum(d * d)};
}
__device__ __forceinline__ void ln_comb(f32x2 p, float& mb, float& rden) {
  const float mean = sum8(p[0]) * (1.f / 512.f), dv = p[0] * (1.f / 64.f) - mean;
  const float var = sum8(fmaf(dv * dv, 64.f, p[1])) * (1.f / 512.f);
  mb = rbf(mean);
  rden = ln_rden(var);
}

struct Lds {
  u32x4 tile[16][512];
  f32x4 zs[8][128];
  u32x4 ms[256];
  float red[16][128];
  u16 hbuf[16][32];
  f32x2 ml[16];
  f32x2 st1[8], st2[8];
  float zpart[4][16];
  f32x2 yred[16];
  float x1d[2], hd[2];
  u32 qs[64];
  u32 ctr, gcnt, zcnt, b2, qflag, rdyflag;
  u16 mpk[64];
};

#define TS(k) do {} while (0)

__global__ void __launch_bounds__(1024, 4)
fused(const u16* __restrict__ x, const u16* __restrict__ Kc, const u16* __restrict__ Vc, const u16* __restrict__ wq, const u16* __restrict__ wo,
      const u16* __restrict__ w1, const u16* __restrict__ b1, const u16* __restrict__ w2, const u16* __restrict__ b2, const u16* __restrict__ cc,
      const u16* __restrict__ wmod, u16* __restrict__ out, unsigned char* ws) {
  __shared__ Lds L;
  TS(0);
  const int tid = threadIdx.x, lane = tid & 63, w = __builtin_amdgcn_readfirstlane(tid >> 6);
  const int r = lane >> 4, c = lane & 15;
  const int bid = blockIdx.x, rho = bid >> 5, u = bid & 31, head = (u & 24) | ((u + rho) & 7);
  const int slot = head * 8 + rho;
  const int grp = head >> 2, cs = (u & 7) * 4 + (head & 3);
  const u64 E = *(const CAS u64*)(ws + WS_E) + EPOCH_STEP;
  const u32 tlo = (u32)E << 24, thi = (u32)(E >> 8);
  gu64* const RDY = (gu64*)(ws + WS_RDY);
  gu64* const MG = (gu64*)(ws + WS_MG);
  gu64* const QG = (gu64*)(ws + WS_QG);
  const __amdgpu_buffer_rsrc_t rws = __builtin_amdgcn_make_buffer_rsrc((void*)ws, 0, WS_END, 0x00020000);

  const int ntile = tile_count(rho);
  const size_t kvbase = ((size_t)head * SK + (size_t)tile_start(rho) * 16) * DH;
  const __amdgpu_buffer_rsrc_t rk = __builtin_amdgcn_make_buffer_rsrc((void*)(Kc + kvbase), 0, ntile * 4096, 0x00020000);
  const __amdgpu_buffer_rsrc_t rv = __builtin_amdgcn_make_buffer_rsrc((void*)(Vc + kvbase), 0, ntile * 4096, 0x00020000);
  const int voff = lane * 16;
  u32x4 kA[4], vA[4], kB[4], vB[4];
#define LDT(dst, rs, t) do { _Pragma("unroll") for (int i_ = 0; i_ < 4; ++i_) dst[i_] = __builtin_amdgcn_raw_buffer_load_b128(rs, voff + i_ * 1024, (t) * 4096, AUX_NT); } while (0)
#define PACE_WAIT() do { if (paced) { paced = !__builtin_amdgcn_readfirstlane(__hip_atomic_load(&L.qflag, __ATOMIC_RELAXED, __HIP_MEMORY_SCOPE_WORKGROUP)); \
      if (paced) asm volatile("s_waitcnt vmcnt(%0)" :: "n"(PACE - 1) : "memory"); } } while (0)
#define DMA1(rs, ldsb, so, OFS) do { PACE_WAIT(); asm volatile("s_mov_b32 m0, %0\n\ts_nop 0\n\tbuffer_load_dwordx4 %1, %2, %3 offen offset:" #OFS " nt lds" \
      :: "s"(ldsb), "v"(voff), "s"(rs), "s"(so) : "memory"); } while (0)
#define DMA4(rs, ldsb, so) do { DMA1(rs, ldsb, so, 0); DMA1(rs, ldsb, so, 1024); DMA1(rs, ldsb, so, 2048); DMA1(rs, ldsb, so, 3072); } while (0)
#define LDP(dst, rs, t) do { _Pragma("unroll") for (int i_ = 0; i_ < 4; ++i_) { PACE_WAIT(); \
    dst[i_] = __builtin_amdgcn_raw_buffer_load_b128(rs, voff + i_ * 1024, (t) * 4096, AUX_NT); asm volatile("" ::: "memory"); } } while (0)
#define WG_BARRIER() asm volatile("s_waitcnt lgkmcnt(0)\n\ts_barrier" ::: "memory")

  const bool mrole = rho == 0 || (rho == 2 && u < 16);
  const int unit = rho == 0 ? u : 32 + u;
  if (tid == 0) { L.ctr = 48; L.gcnt = 0; L.zcnt = 0; L.qflag = 0; L.rdyflag = 0; }
  if (w == 15) {
    const int o = lane < 32 ? WS_AS + slot * 512 + lane * 16 : lane < 36 ? WS_ZS + grp * 2048 + cs * 64 + (lane - 32) * 16
                : (lane < 44 && mrole && unit >= 16) ? WS_MS + (unit - 16) * 128 + (lane - 36) * 16 : WS_END;
    __builtin_amdgcn_raw_buffer_store_b128(u32x4{SENT, SENT, SENT, SENT}, rws, o, 0, AUX_SC1);
    __builtin_amdgcn_raw_buffer_store_b64(u32x2{SENT, SENT}, rws, lane == 0 ? WS_ML + slot * 8 : WS_END, 0, AUX_SC1);
  }
  if (mrole) {
    const __amdgpu_buffer_rsrc_t rm = __builtin_amdgcn_make_buffer_rsrc((void*)wmod, 0, D * 6 * D * 2, 0x00020000);
    const int mo = (32 * w + (lane >> 3)) * (6 * D * 2) + unit * 128 + (lane & 7) * 16;
    u32x4 wm[4];
    const u32 ct = ((const u32*)cc)[16 * w + (lane & 15)];
#pragma unroll
    for (int i = 0; i < 4; ++i) wm[i] = __builtin_amdgcn_raw_buffer_load_b128(rm, mo, i * 8 * (6 * D * 2), AUX_NT);
    const float c0 = bflo(ct), c1 = bfhi(ct);
    const u32 sp = pkbf(c0 * rcp(1.f + ex2(-c0 * LOG2E)), c1 * rcp(1.f + ex2(-c1 * LOG2E)));
    float sv[4];
#pragma unroll
    for (int i = 0; i < 4; ++i) { const u32 t = (u32)__builtin_amdgcn_ds_bpermute((4 * i + (lane >> 4)) * 4, (int)sp); sv[i] = (lane & 8) ? bfhi(t) : bflo(t); }
    f32x2 a[4];
#pragma unroll
    for (int j = 0; j < 4; ++j) {
      a[j] = f32x2{bflo(wm[0][j]), bfhi(wm[0][j])} * sv[0];
#pragma unroll
      for (int i = 1; i < 4; ++i) a[j] += f32x2{bflo(wm[i][j]), bfhi(wm[i][j])} * sv[i];
    }
    const float e0 = rows4(a[0][0], a[0][1], a[1][0], a[1][1]), e1 = rows4(a[2][0], a[2][1], a[3][0], a[3][1]);
    const bool b3 = lane & 8;
    L.red[w][8 * (lane & 7) + ((lane & 8) >> 1) + r] = (b3 ? e1 : e0) + dpp<ROR8>(b3 ? e0 : e1);
    TS(11);
    WG_BARRIER();
    if (w == 15) {
      float p[16];
#pragma unroll
      for (int i = 0; i < 16; ++i) p[i] = L.red[i][lane];
      const float v = rbf((((p[0] + p[1]) + (p[2] + p[3])) + ((p[4] + p[5]) + (p[6] + p[7]))) + (((p[8] + p[9]) + (p[10] + p[11])) + ((p[12] + p[13]) + (p[14] + p[15]))));
      if (unit < 16) st_gran(MG + unit * 64 + lane, v, tlo, thi);
      else {
        L.mpk[lane] = (u16)(asu(v) >> 16);
        asm volatile("s_waitcnt lgkmcnt(0)" ::: "memory");
        u32x4 pk = ((const u32x4*)L.mpk)[lane & 7];
#pragma unroll
        for (int i = 0; i < 4; ++i) pk[i] = nosent(pk[i]);
        __builtin_amdgcn_raw_buffer_store_b128(pk, rws, lane < 8 ? WS_MS + (unit - 16) * 128 + lane * 16 : WS_END, 0, AUX_SC1);
      }
    }
    TS(1);
  }
  WG_BARRIER();
  if (rho == 1) {
    const __amdgpu_buffer_rsrc_t rq = __builtin_amdgcn_make_buffer_rsrc((void*)wq, 0, D * H * DH * 2, 0x00020000);
    u32x4 wqv[8];
    const int qo = (32 * w + r) * 8192 + head * 256 + c * 16;
    const u32x4 xa = *(const u32x4*)(x + lane * 8);
    const float xme = bf2f(x[32 * w + (lane & 31)]);
#pragma unroll
    for (int i = 0; i < 8; ++i) wqv[i] = __builtin_amdgcn_raw_buffer_load_b128(rq, qo, i * 32768, AUX_NT);
    asm volatile("" ::: "memory");
    float xv[8];
#pragma unroll
    for (int i = 0; i < 4; ++i) { xv[2 * i] = bflo(xa[i]); xv[2 * i + 1] = bfhi(xa[i]); }
    const float mean = wave_sum(((xv[0] + xv[1]) + (xv[2] + xv[3])) + ((xv[4] + xv[5]) + (xv[6] + xv[7]))) * (1.f / 512.f);
    float qq = 0.f;
#pragma unroll
    for (int i = 0; i < 8; ++i) { const float d = xv[i] - mean; qq = fmaf(d, d, qq); }
    const float xn = ln_el(xme, rbf(mean), ln_rden(wave_sum(qq) * (1.f / 512.f)));
    u32 wp[4][8];
#pragma unroll
    for (int i = 0; i < 4; ++i)
#pragma unroll
      for (int j = 0; j < 4; ++j) {
        wp[i][2 * j] = __builtin_amdgcn_perm(wqv[2 * i + 1][j], wqv[2 * i][j], 0x05040100u);
        wp[i][2 * j + 1] = __builtin_amdgcn_perm(wqv[2 * i + 1][j], wqv[2 * i][j], 0x07060302u);
      }
#pragma unroll
    for (int i = 0; i < 4; ++i)
      asm volatile("" : "+v"(wp[i][0]), "+v"(wp[i][1]), "+v"(wp[i][2]), "+v"(wp[i][3]), "+v"(wp[i][4]), "+v"(wp[i][5]), "+v"(wp[i][6]), "+v"(wp[i][7]));
    const gu64* gp = MG + (lane >> 5) * 512 + 32 * w + (lane & 31);
    u64 g = 0;
    bool all = false;
    _Pragma("nounroll") for (u32 spin = 0; spin < SPIN_CAP; ++spin) {
      if (all || (lane & 31) == 0) g = ld_gran(gp);
      const u64 okm = __ballot(gran_ok(g, tlo, thi));
      if (okm == ~0ull) break;
      all = (okm & 0x100000001ull) == 0x100000001ull;
      if (!all) __builtin_amdgcn_s_sleep(2);
    }
    TS(1);
    const float mv = gran_val(g);
    auto sw = __builtin_amdgcn_permlane32_swap(asu(mv), asu(mv), false, false);
    const float sa = asf(sw[0]), ca = asf(sw[1]);
    const float hv = rbf(xn * rbf(1.f + ca)) + sa;
    if (lane < 32) L.hbuf[w][(lane & 3) * 8 + (lane >> 2)] = (u16)(pkbf(hv, hv) & 0xffffu);
    asm volatile("s_waitcnt lgkmcnt(0)" ::: "memory");
    const u32x4 hp = *(const u32x4*)&L.hbuf[w][8 * r];
    float a[8];
#pragma unroll
    for (int j = 0; j < 8; ++j) a[j] = dot2(wp[3][j], hp[3], dot2(wp[2][j], hp[2], dot2(wp[1][j], hp[1], dot2(wp[0][j], hp[0], 0.f))));
    L.red[w][8 * c + r] = rows4(a[0], a[1], a[2], a[3]);
    L.red[w][8 * c + 4 + r] = rows4(a[4], a[5], a[6], a[7]);
    WG_BARRIER();
    if (w < 2) {
      float q = 0.f;
#pragma unroll
      for (int i = 0; i < 16; ++i) q += L.red[i][tid];
      q = rbf(q);
      st_gran(QG + head * 128 + tid, q, tlo, thi);
      ((u16*)L.qs)[tid] = (u16)(asu(q) >> 16);
    }
    TS(2);
    WG_BARRIER();
    if (tid == 0) L.qflag = 1;
  } else if (w == 0) {
    u32x4 g = {0u, 0u, 0u, 0u};
    bool all = false;
    _Pragma("nounroll") for (u32 spin = 0; spin < SPIN_CAP; ++spin) {
      if (all || (lane & 31) == 0) g = __builtin_amdgcn_raw_buffer_load_b128(rws, WS_QG + head * 1024 + lane * 16, 0, AUX_SC1);
      const u64 okm = __ballot(g[1] == thi && g[3] == thi && ((g[0] ^ tlo) | (g[2] ^ tlo)) < 0x01000000u);
      if (okm == ~0ull) break;
      all = (okm & 0x100000001ull) == 0x100000001ull;
      if (!all) __builtin_amdgcn_s_sleep(4);
    }
    L.qs[lane] = ((g[0] >> 8) & 0xffffu) | ((g[2] << 8) & 0xffff0000u);
    asm volatile("s_waitcnt lgkmcnt(0)" ::: "memory");
    if (lane == 0) L.qflag = 1;
    TS(2);
  }
  bool paced = rho != 1 && w != 0;
  const u32 tl_addr = (u32)(size_t)(LAS u32x4*)&L.tile[w][0];
  DMA4(rk, tl_addr, w * 4096);
  if (w == 15) {
    asm volatile("s_waitcnt vmcnt(4)" ::: "memory");
    if (lane == 0) __hip_atomic_store(RDY + slot, E, __ATOMIC_RELAXED, __HIP_MEMORY_SCOPE_AGENT);
  }
  DMA4(rv, tl_addr + 4096, w * 4096);
  LDP(kA, rk, 16 + w); LDP(vA, rv, 16 + w); LDP(kB, rk, 32 + w); LDP(vB, rv, 32 + w);
  const u32x4 wov = WOLD((const u32x4*)(wo + (size_t)(grp * 512 + 32 * w + (lane >> 1)) * D + 16 * cs + 8 * (lane & 1)));
  const u16* ap_ = tid < 172 ? w1 + tid * 8 : tid < 344 ? b1 + (tid - 172) * 8 : tid < 516 ? w2 + (tid - 344) * 8 : x + (tid < 580 ? tid - 516 : 0) * 8;
  const u32x4 auxv = *(const u32x4*)ap_;
  const u32 b2raw = b2[0];
  u32x4 rd0, rd1;
#define SAMPLE_RDY() do { rd0 = __builtin_amdgcn_raw_buffer_load_b128(rws, w == 4 ? WS_RDY + lane * 16 : WS_END, 0, AUX_SC1); \
    rd1 = __builtin_amdgcn_raw_buffer_load_b128(rws, w == 4 ? WS_RDY + 1024 + lane * 16 : WS_END, 0, AUX_SC1); } while (0)
#define RDY_OK() __all(((rd0[0] ^ elo) | (rd0[2] ^ elo) | (rd1[0] ^ elo) | (rd1[2] ^ elo) | (rd0[1] ^ ehi) | (rd0[3] ^ ehi) | (rd1[1] ^ ehi) | (rd1[3] ^ ehi)) == 0u)
  const u32 elo = (u32)E, ehi = (u32)(E >> 32);
  SAMPLE_RDY();
  WG_BARRIER();
  TS(3);
  const u32x4 qp = *(const u32x4*)&L.qs[4 * c];

  float m = -INFINITY, l = 0.f;
  f32x2 acc[4] = {{0.f, 0.f}, {0.f, 0.f}, {0.f, 0.f}, {0.f, 0.f}};
  const bool c8 = c & 8, c4 = c & 4;
  const u32 ctr_addr = (u32)(size_t)(LAS u32*)&L.ctr, one = 1u;
#define GRAB(dst) do { u64 sv_; asm volatile("s_mov_b64 %1, exec\n\ts_mov_b64 exec, 1\n\tds_add_rtn_u32 %0, %2, %3\n\ts_mov_b64 exec, %1" \
    : "=&v"(dst), "=&s"(sv_) : "v"(ctr_addr), "v"(one) : "memory"); } while (0)
#define GRAB_WAIT(dst) asm volatile("s_waitcnt lgkmcnt(0)" : "+v"(dst) :: "memory")
#define TILE(kk, vv, tcur, MORE) do { \
    int tv_ = w; if (MORE == 1) GRAB(tv_); \
    float x_[4]; \
    _Pragma("unroll") for (int t_ = 0; t_ < 4; ++t_) { float d_ = dot2(kk[t_][0], qp[0], 0.f); d_ = dot2(kk[t_][1], qp[1], d_); d_ = dot2(kk[t_][2], qp[2], d_); x_[t_] = dot2(kk[t_][3], qp[3], d_); } \
    asm volatile("" : "+v"(x_[0]), "+v"(x_[1]), "+v"(x_[2]), "+v"(x_[3])); \
    __builtin_amdgcn_sched_barrier(0); \
    if (MORE == 1) GRAB_WAIT(tv_); else asm volatile("" ::: "memory"); \
    const int tn_ = __builtin_amdgcn_readfirstlane(tv_); \
    if (MORE == 1) LDT(kk, rk, tn_); \
    if (MORE == 2) { _Pragma("unroll") for (int i_ = 0; i_ < 4; ++i_) kk[i_] = L.tile[w][i_ * 64 + lane]; } \
    asm volatile("" ::: "memory"); __builtin_amdgcn_sched_barrier(0); \
    const float y0_ = (c8 ? x_[2] : x_[0]) + dpp<ROR8>(c8 ? x_[0] : x_[2]); \
    const float y1_ = (c8 ? x_[3] : x_[1]) + dpp<ROR8>(c8 ? x_[1] : x_[3]); \
    float z_ = (c4 ? y1_ : y0_) + dpp<HMIR>(c4 ? y0_ : y1_); \
    z_ += dpp<XOR2>(z_); z_ += dpp<XOR1>(z_); \
    const float s_ = rbf(rbf(z_) * SCALE); \
    float mx_ = fmaxf(s_, dpp<HMIR>(s_)); mx_ = fmaxf(mx_, dpp<ROR8>(mx_)); \
    const float mn_ = fmaxf(m, mx_); \
    const float al_ = ex2((m - mn_) * LOG2E), p_ = ex2((s_ - mn_) * LOG2E); \
    m = mn_; l = fmaf(l, al_, p_); \
    _Pragma("unroll") for (int j_ = 0; j_ < 4; ++j_) acc[j_] *= al_; \
    { const float p0_ = dpp<NBC + 0>(p_), p1_ = dpp<NBC + 4>(p_), p2_ = dpp<NBC + 8>(p_), p3_ = dpp<NBC + 12>(p_); \
      _Pragma("unroll") for (int j_ = 0; j_ < 4; ++j_) { \
        acc[j_] += f32x2{bflo(vv[0][j_]), bfhi(vv[0][j_])} * p0_; acc[j_] += f32x2{bflo(vv[1][j_]), bfhi(vv[1][j_])} * p1_; \
        acc[j_] += f32x2{bflo(vv[2][j_]), bfhi(vv[2][j_])} * p2_; acc[j_] += f32x2{bflo(vv[3][j_]), bfhi(vv[3][j_])} * p3_; } } \
    asm volatile("" : "+v"(acc[0]), "+v"(acc[1]), "+v"(acc[2]), "+v"(acc[3]), "+v"(l), "+v"(m)); \
    __builtin_amdgcn_sched_barrier(0); \
    if (MORE == 1) LDT(vv, rv, tn_); \
    if (MORE == 2) { _Pragma("unroll") for (int i_ = 0; i_ < 4; ++i_) vv[i_] = L.tile[w][256 + i_ * 64 + lane]; } \
    asm volatile("" ::: "memory"); __builtin_amdgcn_sched_barrier(0); \
    tcur = tn_; } while (0)
  int ta = 16 + w, tb = 32 + w;
  TILE(kA, vA, ta, 2);
  TILE(kB, vB, tb, 1);
  {
    u16* wf = (u16*)&L.tile[w][(lane >> 4) * 16 + (lane & 1) * 8] + ((lane >> 1) & 7);
#pragma unroll
    for (int j = 0; j < 4; ++j) { wf[16 * j] = (u16)(wov[j] & 0xffffu); wf[16 * j + 8] = (u16)(wov[j] >> 16); }
    L.tile[w][64 + lane] = auxv;
    if (tid == 0) L.b2 = b2raw;
    if (RDY_OK() && lane == 0) L.rdyflag = 1;
    asm volatile("" ::: "memory");
  }
  for (int it = 0; it < 256 && (u32)tb < (u32)ntile; ++it) {
    TILE(kA, vA, ta, 1);
    TILE(kB, vB, tb, 1);
  }
  if ((u32)ta < (u32)ntile) TILE(kA, vA, ta, 0);
  TS(4);

  {
    float lr = l + dpp<HMIR>(l); lr += dpp<ROR8>(lr);
    const float Mw = xrow_max(m), sc = ex2((m - Mw) * LOG2E);
    lr = xrow_sum(lr * sc);
#pragma unroll
    for (int j = 0; j < 4; ++j) acc[j] *= sc;
    L.red[w][8 * c + r] = rows4(acc[0][0], acc[0][1], acc[1][0], acc[1][1]);
    L.red[w][8 * c + 4 + r] = rows4(acc[2][0], acc[2][1], acc[3][0], acc[3][1]);
    if (lane == 0) L.ml[w] = f32x2{Mw, lr};
  }
  WG_BARRIER();
  TS(5);
#define LDS_WAIT(word, n) do { _Pragma("nounroll") for (u32 spin_ = 0; spin_ < (1u << 22); ++spin_) \
    if (__hip_atomic_load(&(word), __ATOMIC_RELAXED, __HIP_MEMORY_SCOPE_WORKGROUP) >= (n)) break; \
    asm volatile("" ::: "memory"); } while (0)
#define LDS_SIGNAL(word) do { asm volatile("s_waitcnt lgkmcnt(0)" ::: "memory"); \
    if (lane == 0) __hip_atomic_fetch_add(&(word), 1u, __ATOMIC_RELAXED, __HIP_MEMORY_SCOPE_WORKGROUP); } while (0)
  const int dd = (tid - 512) & 511;
  if (w < 4) {
    if (w == 0) {
      const f32x2 mlv = L.ml[c];
      f32x2 pv[16];
#pragma unroll
      for (int i = 0; i < 16; ++i) pv[i] = *(const f32x2*)&L.red[i][2 * lane];
      float M = fmaxf(mlv[0], dpp<XOR1>(mlv[0])); M = fmaxf(M, dpp<XOR2>(M)); M = fmaxf(M, dpp<HMIR>(M)); M = fmaxf(M, dpp<ROR8>(M));
      const float wt = ex2((mlv[0] - M) * LOG2E);
      float ls = sum8(mlv[1] * wt); ls += dpp<ROR8>(ls);
      f32x2 o0 = pv[0] * dpp<NBC + 0>(wt), o1 = pv[1] * dpp<NBC + 1>(wt), o2 = pv[2] * dpp<NBC + 2>(wt), o3 = pv[3] * dpp<NBC + 3>(wt);
#define MRG(i) o0 += pv[i] * dpp<NBC + i>(wt); o1 += pv[i + 1] * dpp<NBC + i + 1>(wt); o2 += pv[i + 2] * dpp<NBC + i + 2>(wt); o3 += pv[i + 3] * dpp<NBC + i + 3>(wt);
      MRG(4) MRG(8) MRG(12)
      const f32x2 o = (o0 + o1) + (o2 + o3);
      const u32 hi = nosent(pkbf(o[0], o[1])), lo = nosent(pkbf(o[0] - bflo(hi), o[1] - bfhi(hi)));
      const float sel = asf((lane & 1) ? hi : lo), own = asf((lane & 1) ? lo : hi);
      const float nb = dpp<XOR1>(sel);
      const float e0 = (lane & 1) ? nb : own, e1 = (lane & 1) ? own : nb;
      const u32x4 pc = {asu(e0), asu(e1), asu(dpp<XOR2>(e0)), asu(dpp<XOR2>(e1))};
      __builtin_amdgcn_raw_buffer_store_b128(pc, rws, (lane & 2) ? WS_END : WS_AS + slot * 512 + (lane & 1) * 256 + (lane >> 2) * 16, 0, AUX_SC1);
      __builtin_amdgcn_raw_buffer_store_b64(u32x2{nosent(asu(M)), nosent(asu(ls))}, rws, lane == 0 ? WS_ML + slot * 8 : WS_END, 0, AUX_SC1);
      TS(10);
    }
    u32x4 bfr[4];
#pragma unroll
    for (int ks = 0; ks < 4; ++ks) bfr[ks] = L.tile[4 * w + ks][lane];
    LDS_WAIT(L.rdyflag, 1u);
    const int ps = (grp * 4 + w) * 8 + (lane & 7);
    const int ao = WS_AS + ps * 512 + (lane & 8) * 32 + r * 16;
    u32x4 af[4];
    u32x2 mlp;
    bool ok = false;
    _Pragma("nounroll") for (u32 spin = 0; spin < SPIN_CAP; ++spin) {
      if (!ok) {
#pragma unroll
        for (int ks = 0; ks < 4; ++ks) af[ks] = __builtin_amdgcn_raw_buffer_load_b128(rws, ao + 64 * ks, 0, AUX_SC1);
        mlp = __builtin_amdgcn_raw_buffer_load_b64(rws, WS_ML + ps * 8, 0, AUX_SC1);
        ok = ok4(af[0]) && ok4(af[1]) && ok4(af[2]) && ok4(af[3]) && max(mlp[0], mlp[1]) != SENT;
      }
      const u64 miss = __ballot(!ok);
      if (!miss) break;
      if (__builtin_popcountll(miss) > 16) __builtin_amdgcn_s_sleep(24); else __builtin_amdgcn_s_sleep(2);
    }
    TS(6);
    f32x4 d4 = {0.f, 0.f, 0.f, 0.f};
#pragma unroll
    for (int ks = 0; ks < 4; ++ks) d4 = __builtin_amdgcn_mfma_f32_16x16x32_bf16(__builtin_bit_cast(bf16x8, af[ks]), __builtin_bit_cast(bf16x8, bfr[ks]), d4, 0, 0, 0);
    const float Ms = asf(mlp[0]);
    float Mx = fmaxf(Ms, dpp<XOR1>(Ms)); Mx = fmaxf(Mx, dpp<XOR2>(Mx)); Mx = fmaxf(Mx, dpp<HMIR>(Mx));
    const float wt = ex2((Ms - Mx) * LOG2E);
    const float cf = wt * rcp(sum8(wt * asf(mlp[1])));
    const float cs4 = (r & 1) ? dpp<HMIR>(cf) : cf;
    const bool od = r & 1;
    const float zp = xrow_sum(fmaf(od ? dpp<NBC + 3>(cs4) : dpp<NBC + 0>(cs4), d4[0], (od ? dpp<NBC + 2>(cs4) : dpp<NBC + 1>(cs4)) * d4[1]) +
                              fmaf(od ? dpp<NBC + 1>(cs4) : dpp<NBC + 2>(cs4), d4[2], (od ? dpp<NBC + 0>(cs4) : dpp<NBC + 3>(cs4)) * d4[3]));
    if (lane < 16) L.zpart[w][lane] = zp;
    LDS_SIGNAL(L.zcnt);
    if (w == 0) {
      LDS_WAIT(L.zcnt, 4u);
      const f32x4 z = (*(const f32x4*)&L.zpart[0][4 * (lane & 3)] + *(const f32x4*)&L.zpart[1][4 * (lane & 3)]) +
                      (*(const f32x4*)&L.zpart[2][4 * (lane & 3)] + *(const f32x4*)&L.zpart[3][4 * (lane & 3)]);
      const u32x4 zz = {nosent(asu(z[0])), nosent(asu(z[1])), nosent(asu(z[2])), nosent(asu(z[3]))};
      __builtin_amdgcn_raw_buffer_store_b128(zz, rws, lane < 4 ? WS_ZS + grp * 2048 + cs * 64 + lane * 16 : WS_END, 0, AUX_SC1);
    }
    TS(7);
  } else if (w == 4) {
    if (!__builtin_amdgcn_readfirstlane(L.rdyflag)) {
      _Pragma("nounroll") for (u32 spin = 0; spin < SPIN_CAP; ++spin) { SAMPLE_RDY(); if (RDY_OK()) break; }
      if (lane == 0) L.rdyflag = 1;
    }
    u32x4 mv[4];
    _Pragma("nounroll") for (u32 spin = 0; spin < SPIN_CAP; ++spin) {
#pragma unroll
      for (int i = 0; i < 4; ++i) mv[i] = __builtin_amdgcn_raw_buffer_load_b128(rws, WS_MS + i * 1024 + lane * 16, 0, AUX_SC1);
      if (__all(ok4(mv[0]) && ok4(mv[1]) && ok4(mv[2]) && ok4(mv[3]))) break;
      __builtin_amdgcn_s_sleep(8);
    }
#pragma unroll
    for (int i = 0; i < 4; ++i) L.ms[i * 64 + lane] = mv[i];
    LDS_SIGNAL(L.gcnt);
  }
#define AUX32(i) (((const u32*)&L.tile[(i) >> 8][64])[(i) & 255])
#define AUX16(i) (((const u16*)&L.tile[(i) >> 9][64])[(i) & 511])
  float x1 = 0.f, xsb = 0.f, xcb = 0.f;
  if (w >= 8) {
    LDS_WAIT(L.rdyflag, 1u);
    const int zo = WS_ZS + (w - 8) * 2048 + lane * 16;
    u32x4 z0, z1;
    bool ok0 = false, ok1 = false;
    _Pragma("nounroll") for (u32 spin = 0; spin < SPIN_CAP; ++spin) {
      if (!ok0) z0 = __builtin_amdgcn_raw_buffer_load_b128(rws, zo, 0, AUX_SC1);
      if (!ok1) z1 = __builtin_amdgcn_raw_buffer_load_b128(rws, zo + 1024, 0, AUX_SC1);
      ok0 = ok4(z0); ok1 = ok4(z1);
      const u64 miss = __ballot(!(ok0 && ok1));
      if (!miss) break;
      if (__builtin_popcountll(miss) > 48) __builtin_amdgcn_s_sleep(16); else __builtin_amdgcn_s_sleep(2);
    }
    L.zs[w - 8][lane] = __builtin_bit_cast(f32x4, z0);
    L.zs[w - 8][64 + lane] = __builtin_bit_cast(f32x4, z1);
    LDS_SIGNAL(L.gcnt);
    const float xv = bf2f(AUX16(3 * F + dd));
    LDS_WAIT(L.gcnt, 9u);
    TS(6);
    const float* zf = (const float*)L.zs + dd;
    const u16* msv = (const u16*)L.ms + dd;
    const float at = ((zf[0] + zf[512]) + (zf[1024] + zf[1536])) + ((zf[2048] + zf[2560]) + (zf[3072] + zf[3584]));
    x1 = rbf(xv + rbf(bf2f(msv[0]) * rbf(at)));
    xsb = bf2f(msv[512]); xcb = rbf(1.f + bf2f(msv[1024]));
    const f32x2 p = ln_part(x1);
    if (lane == 0) L.st1[w - 8] = p;
  }
  const int fp = min(tid, F / 2 - 1);
  const u32 pw1 = AUX32(fp), pb1 = AUX32(F / 2 + fp), pw2 = tid < F / 2 ? AUX32(F + fp) : 0u;
  WG_BARRIER();
  TS(8);
  if (w >= 8) {
    float mb, rden;
    ln_comb(L.st1[lane & 7], mb, rden);
    const float hh = rbf(rbf(ln_el(x1, mb, rden) * xcb) + xsb);
    if ((dd >> 1) == bid) { L.x1d[dd & 1] = x1; L.hd[dd & 1] = hh; }
    const f32x2 p = ln_part(hh);
    if (lane == 0) L.st2[w - 8] = p;
  }
  WG_BARRIER();
  TS(9);
  if (w < 11) {
    float mb, rden;
    ln_comb(L.st2[lane & 7], mb, rden);
    const f32x2 w1f = {bflo(pw1), bfhi(pw1)}, b1f = {bflo(pb1), bfhi(pb1)};
    f32x2 y;
#pragma unroll
    for (int k = 0; k < 2; ++k) {
      const float t = ln_el(L.hd[k], mb, rden);
      const f32x2 tw = w1f * t;
      const u32 twb = pkbf(tw[0], tw[1]);
      const f32x2 us = f32x2{bflo(twb), bfhi(twb)} + b1f;
      const u32 ub = pkbf(us[0], us[1]);
      const f32x2 uu = {bflo(ub), bfhi(ub)};
      constexpr float KK = -2.f * 0.7978845608028654f * LOG2E;
      const f32x2 zz = uu * (uu * uu * (0.044715f * KK) + KK);
      const f32x2 sg = {rcp(1.f + ex2(zz[0])), rcp(1.f + ex2(zz[1]))};
      const f32x2 gg = uu * sg;
      y[k] = wave_sum(dot2(pkbf(gg[0], gg[1]), pw2, 0.f));
    }
    if (lane == 0) L.yred[w] = y;
  }
  WG_BARRIER();
  if (w == 0) {
    const int k = r & 1;
    float y = c < 11 ? L.yred[c][k] : 0.f;
    y = sum8(y); y += dpp<ROR8>(y);
    if (lane == 0 || lane == 16) {
      const float yy = rbf(rbf(y) + asf(L.b2 << 16));
      out[2 * bid + k] = (u16)(pkbf(L.x1d[k] + rbf(bf2f(((const u16*)L.ms)[1536 + 2 * bid + k]) * yy), 0.f) & 0xffffu);
    }
    if (bid == 0 && lane == 0) __hip_atomic_store((gu64*)(ws + WS_E), E, __ATOMIC_RELAXED, __HIP_MEMORY_SCOPE_AGENT);
  }
}

extern "C" void kernel_launch(void* const* d_in, const int* in_sizes, int n_in, void* d_out, int out_size, void* d_ws, size_t ws_size, hipStream_t stream) {
  fused<<<256, 1024, 0, stream>>>((const u16*)d_in[0], (const u16*)d_in[1], (const u16*)d_in[2], (const u16*)d_in[3], (const u16*)d_in[4],
                                  (const u16*)d_in[6], (const u16*)d_in[7], (const u16*)d_in[8], (const u16*)d_in[9], (const u16*)d_in[10],
                                  (const u16*)d_in[11], (u16*)d_out, (unsigned char*)d_ws);
}
```

```cpp
#include <hip/hip_runtime.h>
#include <hip/hip_bf16.h>
#include <stdint.h>

typedef unsigned u32;
typedef unsigned short u16;
typedef unsigned long long u64;
typedef u32 u32x4 __attribute__((ext_vector_type(4)));
typedef u32 u32x2 __attribute__((ext_vector_type(2)));
typedef float f32x4 __attribute__((ext_vector_type(4)));
typedef float f32x2 __attribute__((ext_vector_type(2)));
typedef __bf16 bf16x2 __attribute__((ext_vector_type(2)));
typedef __bf16 bf16x8 __attribute__((ext_vector_type(8)));
#define GAS __attribute__((address_space(1)))
#define CAS __attribute__((address_space(4)))
#define LAS __attribute__((address_space(3)))
typedef GAS u64 gu64;
typedef GAS u32 gu32;

#define PACE 6
#define QUIET 0
#define WONT 1
#define WOLD(p) __builtin_nontemporal_load(p)
#define AUXK 2
#define AUXV 0
#define POL_rk " nt"
#define POL_rv ""
#define NSW 6
#define LEAD (3 * NSW)
#define NLM 88
#define NLQ 80
#define NLR 100

constexpr int D = 512, H = 32, SK = 16384, DH = 128, F = 1376;
constexpr float LOG2E = 1.4426950408889634f;
constexpr float SCALE = 0.08838834764831845f;
constexpr u64 EPOCH_STEP = 0x9E3779B97F4A7C15ull;
constexpr u32 SPIN_CAP = 1u << 13;
constexpr int AUX_NT = 2, AUX_SC1 = 16;
constexpr u32 SENT = 0xFFFFFFFFu;

constexpr int POOL0 = NLM + 2 * NLQ + 5 * NLR;
static_assert(NLQ >= 48 && NLM >= 48 && NLR >= 48 && POOL0 <= 1024 && NLQ % 2 == 0 && NLM % 2 == 0 && NLR % 2 == 0, "three fixed tiles per wave, then pairs");
__device__ __forceinline__ int tile_start(int rho) {
  return rho == 0 ? 0 : rho == 1 ? NLM : rho == 2 ? NLM + NLQ : rho == 3 ? NLM + NLQ + NLR : NLM + 2 * NLQ + (rho - 3) * NLR;
}
__device__ __forceinline__ int tile_count(int rho) { return rho == 0 ? NLM : (rho == 1 || rho == 3) ? NLQ : NLR; }

constexpr int WS_E = 0;
constexpr int WS_RDY = 4096;
constexpr int WS_MG = 8192;
constexpr int WS_MS = 16384;
constexpr int WS_PC = 24576;
constexpr int WS_QG = 32768;
constexpr int WS_ML = 65536;
constexpr int WS_ZS = 69632;
constexpr int WS_AS = 98304;
constexpr int WS_END = WS_AS + 256 * 512;

__device__ __forceinline__ float asf(u32 u) { return __builtin_bit_cast(float, u); }
__device__ __forceinline__ u32 asu(float f) { return __builtin_bit_cast(u32, f); }
__device__ __forceinline__ float bflo(u32 u) { return asf(u << 16); }
__device__ __forceinline__ float bfhi(u32 u) { return asf(u & 0xffff0000u); }
__device__ __forceinline__ float bf2f(u16 b) { return asf((u32)b << 16); }
__device__ __forceinline__ float rbf(float x) { return (float)(__bf16)x; }
__device__ __forceinline__ u32 pkbf(float lo, float hi) { return __builtin_bit_cast(u32, bf16x2{(__bf16)lo, (__bf16)hi}); }
__device__ __forceinline__ float dot2(u32 k, u32 q, float acc) {
  return __builtin_amdgcn_fdot2_f32_bf16(__builtin_bit_cast(bf16x2, k), __builtin_bit_cast(bf16x2, q), acc, false);
}
template <int CTRL> __device__ __forceinline__ float dpp(float x) {
  return asf((u32)__builtin_amdgcn_mov_dpp((int)asu(x), CTRL, 0xf, 0xf, true));
}
constexpr int XOR1 = 0xB1, XOR2 = 0x4E, HMIR = 0x141, ROR8 = 0x128, ROR4 = 0x124, NBC = 0x150;
__device__ __forceinline__ float x16_max(float x) {
  auto s = __builtin_amdgcn_permlane16_swap(asu(x), asu(x), false, false);
  return fmaxf(asf(s[0]), asf(s[1]));
}
__device__ __forceinline__ float x16_sum(float x) {
  auto s = __builtin_amdgcn_permlane16_swap(asu(x), asu(x), false, false);
  return asf(s[0]) + asf(s[1]);
}
__device__ __forceinline__ float xrow_max(float x) {
  x = x16_max(x);
  auto t = __builtin_amdgcn_permlane32_swap(asu(x), asu(x), false, false);
  return fmaxf(asf(t[0]), asf(t[1]));
}
__device__ __forceinline__ float xrow_sum(float x) {
  x = x16_sum(x);
  auto t = __builtin_amdgcn_permlane32_swap(asu(x), asu(x), false, false);
  return asf(t[0]) + asf(t[1]);
}
__device__ __forceinline__ float tr16(float a, float b) {
  auto s = __builtin_amdgcn_permlane16_swap(asu(a), asu(b), false, false);
  return asf(s[0]) + asf(s[1]);
}
__device__ __forceinline__ float tr32(float a, float b) {
  auto s = __builtin_amdgcn_permlane32_swap(asu(a), asu(b), false, false);
  return asf(s[0]) + asf(s[1]);
}
__device__ __forceinline__ float rows4(float a0, float a1, float a2, float a3) { return tr32(tr16(a0, a1), tr16(a2, a3)); }
__device__ __forceinline__ float sum8(float x) { x += dpp<XOR1>(x); x += dpp<XOR2>(x); return x + dpp<HMIR>(x); }
__device__ __forceinline__ float wave_sum(float x) { x = sum8(x); x += dpp<ROR8>(x); return xrow_sum(x); }
__device__ __forceinline__ float ex2(float x) { return __builtin_amdgcn_exp2f(x); }
__device__ __forceinline__ float rcp(float x) { return __builtin_amdgcn_rcpf(x); }

__device__ __forceinline__ bool gran_ok(u32x4 g, u32 tag) { return g[1] == tag && g[3] == tag; }
__device__ __forceinline__ u32 nosent(u32 v) { return v == SENT ? 0xFFFEFFFEu : v; }
__device__ __forceinline__ void st32(gu32* p, u32 v) { __hip_atomic_store(p, nosent(v), __ATOMIC_RELAXED, __HIP_MEMORY_SCOPE_AGENT); }
__device__ __forceinline__ u32 ld32(const gu32* p) { return __hip_atomic_load(p, __ATOMIC_RELAXED, __HIP_MEMORY_SCOPE_AGENT); }
__device__ __forceinline__ bool ok4(u32x4 v) { return max(max(v[0], v[1]), max(v[2], v[3])) != SENT; }

__device__ __forceinline__ float ln_el(float x, float mb, float rden) { return rbf(rbf(x - mb) * rden); }
__device__ __forceinline__ float ln_rden(float var) { return rcp(rbf(__builtin_amdgcn_sqrtf(rbf(rbf(var) + 1e-5f)))); }
__device__ __forceinline__ f32x2 ln_part(float v) {
  const float s = wave_sum(v), d = v - s * (1.f / 64.f);
  return f32x2{s, wave_sum(d * d)};
}
__device__ __forceinline__ void ln_comb(f32x2 p, float& mb, float& rden) {
  const float mean = sum8(p[0]) * (1.f / 512.f), dv = p[0] * (1.f / 64.f) - mean;
  const float var = sum8(fmaf(dv * dv, 64.f, p[1])) * (1.f / 512.f);
  mb = rbf(mean);
  rden = ln_rden(var);
}

struct Lds {
  u32x4 tile[16][512];
  f32x4 zs[8][128];
  u32x4 ms[256];
  float red[16][128];
  u32x2 hbuf[16][8];
  f32x2 ml[16];
  f32x2 st1[8], st2[8];
  float zpart[4][16];
  f32x2 yred[16];
  float x1d[2], hd[2];
  u32 qs[64];
  u32 ctr, gcnt, zcnt, b2, qflag, rdyflag;
  u32x4 mpk[8];
};

#define TS(k) do {} while (0)

__global__ void __launch_bounds__(1024, 4)
fused(const u16* __restrict__ x, const u16* __restrict__ Kc, const u16* __restrict__ Vc, const u16* __restrict__ wq, const u16* __restrict__ wo,
      const u16* __restrict__ w1, const u16* __restrict__ b1, const u16* __restrict__ w2, const u16* __restrict__ b2, const u16* __restrict__ cc,
      const u16* __restrict__ wmod, u16* __restrict__ out, unsigned char* ws) {
  __shared__ Lds L;
  TS(0);
  const int tid = threadIdx.x, lane = tid & 63, w = __builtin_amdgcn_readfirstlane(tid >> 6);
  const int r = lane >> 4, c = lane & 15;
  const int bid = blockIdx.x, rho = bid >> 5, u = bid & 31, head = (u & 24) | ((u + rho) & 7);
  const int slot = head * 8 + rho;
  const int grp = head >> 2, cs = (u & 7) * 4 + (head & 3);
  const u64 E = *(const CAS u64*)(ws + WS_E) + EPOCH_STEP;
  const u32 tag = (u32)E;
  gu64* const RDY = (gu64*)(ws + WS_RDY);
  const __amdgpu_buffer_rsrc_t rws = __builtin_amdgcn_make_buffer_rsrc((void*)ws, 0, WS_END, 0x00020000);

  const int t0 = tile_start(rho), tend = t0 + tile_count(rho);
  const size_t kvbase = (size_t)head * SK * DH;
  const __amdgpu_buffer_rsrc_t rk = __builtin_amdgcn_make_buffer_rsrc((void*)(Kc + kvbase), 0, SK * DH * 2, 0x00020000);
  const __amdgpu_buffer_rsrc_t rv = __builtin_amdgcn_make_buffer_rsrc((void*)(Vc + kvbase), 0, SK * DH * 2, 0x00020000);
  const int voff = lane * 16;
  u32x4 kA[4], vA[4], kB[4], vB[4];
#define AUX_rk AUXK
#define AUX_rv AUXV
#define LDT(dst, rs, t) do { _Pragma("unroll") for (int i_ = 0; i_ < 4; ++i_) dst[i_] = __builtin_amdgcn_raw_buffer_load_b128(rs, voff + i_ * 1024, (t) * 4096, AUX_##rs); } while (0)
#define PACE_WAIT() do { if (paced) { paced = !__builtin_amdgcn_readfirstlane(__hip_atomic_load(&L.qflag, __ATOMIC_RELAXED, __HIP_MEMORY_SCOPE_WORKGROUP)); \
      if (paced) asm volatile("s_waitcnt vmcnt(%0)" :: "n"(PACE - 1) : "memory"); } } while (0)
#define DMA1(rs, ldsb, so, OFS) do { PACE_WAIT(); asm volatile("s_mov_b32 m0, %0\n\ts_nop 0\n\tbuffer_load_dwordx4 %1, %2, %3 offen offset:" #OFS POL_##rs " lds" \
      :: "s"(ldsb), "v"(voff), "s"(rs), "s"(so) : "memory"); } while (0)
#define DMA4(rs, ldsb, so) do { DMA1(rs, ldsb, so, 0); DMA1(rs, ldsb, so, 1024); DMA1(rs, ldsb, so, 2048); DMA1(rs, ldsb, so, 3072); } while (0)
#define LDP(dst, rs, t) do { _Pragma("unroll") for (int i_ = 0; i_ < 4; ++i_) { PACE_WAIT(); \
    dst[i_] = __builtin_amdgcn_raw_buffer_load_b128(rs, voff + i_ * 1024, (t) * 4096, AUX_##rs); asm volatile("" ::: "memory"); } } while (0)
#define WG_BARRIER() asm volatile("s_waitcnt lgkmcnt(0)\n\ts_barrier" ::: "memory")

  const bool qrole = rho == 1 || rho == 3, mrole = rho == 0 || qrole;
  const int unit = ((rho + 1) >> 1) * 32 + u;
  const int qh = rho >> 1;
  u32x4 wqv[4];
  if (qrole) {
    const __amdgpu_buffer_rsrc_t rq = __builtin_amdgcn_make_buffer_rsrc((void*)wq, 0, D * H * DH * 2, 0x00020000);
    const int qo = (32 * w + (lane >> 3)) * 8192 + head * 256 + qh * 128 + (lane & 7) * 16;
#pragma unroll
    for (int i = 0; i < 4; ++i) wqv[i] = __builtin_amdgcn_raw_buffer_load_b128(rq, qo, i * 65536, AUX_NT);
    asm volatile("" ::: "memory");
  }
  if (tid == 0) { L.ctr = t0 + 48; L.gcnt = 0; L.zcnt = 0; L.qflag = 0; L.rdyflag = 0; }
  if (w == 15) {
    const int o = lane < 32 ? WS_AS + slot * 512 + lane * 16 : lane < 36 ? WS_ZS + grp * 2048 + cs * 64 + (lane - 32) * 16
                : (lane < 40 && mrole && unit >= 32) ? WS_MS + (unit - 32) * 64 + (lane - 36) * 16 : WS_END;
    __builtin_amdgcn_raw_buffer_store_b128(u32x4{SENT, SENT, SENT, SENT}, rws, o, 0, AUX_SC1);
    __builtin_amdgcn_raw_buffer_store_b64(u32x2{lane ? 0u : SENT, lane ? 0u : SENT}, rws,
                                          lane == 0 ? WS_ML + slot * 8 : (lane == 1 && rho == 1) ? WS_PC + head * 128 : WS_END, 0, AUX_SC1);
  }
#define SUM16(p) ((((p[0] + p[1]) + (p[2] + p[3])) + ((p[4] + p[5]) + (p[6] + p[7]))) + (((p[8] + p[9]) + (p[10] + p[11])) + ((p[12] + p[13]) + (p[14] + p[15]))))
  if (mrole) {
    const __amdgpu_buffer_rsrc_t rm = __builtin_amdgcn_make_buffer_rsrc((void*)wmod, 0, D * 6 * D * 2, 0x00020000);
    const int mo = (32 * w + (lane >> 2)) * (6 * D * 2) + unit * 64 + (lane & 3) * 16;
    const u32 c0r = cc[32 * w + (lane >> 2)], c1r = cc[32 * w + 16 + (lane >> 2)];
    const u32x4 wm0 = __builtin_amdgcn_raw_buffer_load_b128(rm, mo, 0, AUX_NT), wm1 = __builtin_amdgcn_raw_buffer_load_b128(rm, mo, 16 * (6 * D * 2), AUX_NT);
    const float c0 = bflo(c0r), c1 = bflo(c1r);
    const float s0 = rbf(c0 * rcp(1.f + ex2(-c0 * LOG2E))), s1 = rbf(c1 * rcp(1.f + ex2(-c1 * LOG2E)));
    f32x2 a[4];
#pragma unroll
    for (int j = 0; j < 4; ++j) a[j] = f32x2{bflo(wm0[j]), bfhi(wm0[j])} * s0 + f32x2{bflo(wm1[j]), bfhi(wm1[j])} * s1;
    const float e0 = rows4(a[0][0], a[0][1], a[1][0], a[1][1]), e1 = rows4(a[2][0], a[2][1], a[3][0], a[3][1]);
    const bool b3 = lane & 8;
    L.red[w][(lane & 4) * 8 + 8 * (lane & 3) + ((lane & 8) >> 1) + r] = (b3 ? e1 : e0) + dpp<ROR8>(b3 ? e0 : e1);
    WG_BARRIER();
    if (w == 15) {
      float p[16];
#pragma unroll
      for (int i = 0; i < 16; ++i) p[i] = L.red[i][lane];
      float v = SUM16(p);
      auto sw = __builtin_amdgcn_permlane32_swap(asu(v), asu(v), false, false);
      v = asf(sw[0]) + asf(sw[1]);
      const float v1 = dpp<XOR1>(v);
      const u32 pr = pkbf(v, v1);
      const u32 pr2 = asu(dpp<XOR2>(asf(pr)));
      if (unit < 32) {
        __builtin_amdgcn_raw_buffer_store_b128(u32x4{pr, tag, pr2, tag}, rws, (lane < 32 && !(lane & 3)) ? WS_MG + unit * 128 + lane * 4 : WS_END, 0, AUX_SC1);
      } else {
        __builtin_amdgcn_raw_buffer_store_b64(u32x2{nosent(pr), nosent(pr2)}, rws, (lane < 32 && !(lane & 3)) ? WS_MS + (unit - 32) * 64 + lane * 2 : WS_END, 0, AUX_SC1);
      }
    }
    TS(1);
  }
  WG_BARRIER();
  if (qrole) {
    const u32x4 xa = *(const u32x4*)(x + lane * 8);
    const u32x2 xme = *(const u32x2*)(x + 32 * w + 4 * (lane & 7));
    float xv[8];
#pragma unroll
    for (int i = 0; i < 4; ++i) { xv[2 * i] = bflo(xa[i]); xv[2 * i + 1] = bfhi(xa[i]); }
    const float mean = wave_sum(((xv[0] + xv[1]) + (xv[2] + xv[3])) + ((xv[4] + xv[5]) + (xv[6] + xv[7]))) * (1.f / 512.f);
    float qq = 0.f;
#pragma unroll
    for (int i = 0; i < 8; ++i) { const float d = xv[i] - mean; qq = fmaf(d, d, qq); }
    const float mb = rbf(mean), rden = ln_rden(wave_sum(qq) * (1.f / 512.f));
    float xn[4] = {ln_el(bflo(xme[0]), mb, rden), ln_el(bfhi(xme[0]), mb, rden), ln_el(bflo(xme[1]), mb, rden), ln_el(bfhi(xme[1]), mb, rden)};
    u32 wp[2][8];
#pragma unroll
    for (int i = 0; i < 2; ++i)
#pragma unroll
      for (int j = 0; j < 4; ++j) {
        wp[i][2 * j] = __builtin_amdgcn_perm(wqv[2 * i + 1][j], wqv[2 * i][j], 0x05040100u);
        wp[i][2 * j + 1] = __builtin_amdgcn_perm(wqv[2 * i + 1][j], wqv[2 * i][j], 0x07060302u);
      }
#pragma unroll
    for (int i = 0; i < 2; ++i)
      asm volatile("" : "+v"(wp[i][0]), "+v"(wp[i][1]), "+v"(wp[i][2]), "+v"(wp[i][3]), "+v"(wp[i][4]), "+v"(wp[i][5]), "+v"(wp[i][6]), "+v"(wp[i][7]));
    asm volatile("" : "+v"(xn[0]), "+v"(xn[1]), "+v"(xn[2]), "+v"(xn[3]));
    const int go = lane < 16 ? WS_MG + (lane >> 3) * 2048 + w * 128 + (lane & 7) * 16 : WS_END;
    u32x4 g;
    _Pragma("nounroll") for (u32 spin = 0; spin < SPIN_CAP; ++spin) {
      g = __builtin_amdgcn_raw_buffer_load_b128(rws, go, 0, AUX_SC1);
      if ((__ballot(gran_ok(g, tag)) & 0xffffull) == 0xffffull) break;
      __builtin_amdgcn_s_sleep(3);
    }
    TS(1);
    const u32 ca0 = asu(dpp<ROR8>(asf(g[0]))), ca1 = asu(dpp<ROR8>(asf(g[2])));
    const float h0 = rbf(xn[0] * rbf(1.f + bflo(ca0))) + bflo(g[0]), h1 = rbf(xn[1] * rbf(1.f + bfhi(ca0))) + bfhi(g[0]);
    const float h2 = rbf(xn[2] * rbf(1.f + bflo(ca1))) + bflo(g[2]), h3 = rbf(xn[3] * rbf(1.f + bfhi(ca1))) + bfhi(g[2]);
    if (lane < 8) {
      u16* hb = (u16*)&L.hbuf[w][4 * (lane & 1)] + (lane >> 1);
      const u32 p01 = pkbf(h0, h1), p23 = pkbf(h2, h3);
      hb[0] = (u16)p01; hb[4] = (u16)(p01 >> 16); hb[8] = (u16)p23; hb[12] = (u16)(p23 >> 16);
    }
    asm volatile("s_waitcnt lgkmcnt(0)" ::: "memory");
    const u32x2 hp = L.hbuf[w][lane >> 3];
    float a[8];
#pragma unroll
    for (int j = 0; j < 8; ++j) a[j] = dot2(wp[1][j], hp[1], dot2(wp[0][j], hp[0], 0.f));
    const float e0 = rows4(a[0], a[1], a[2], a[3]), e1 = rows4(a[4], a[5], a[6], a[7]);
    const bool b3 = lane & 8;
    L.red[w][8 * (lane & 7) + ((lane & 8) >> 1) + r] = (b3 ? e1 : e0) + dpp<ROR8>(b3 ? e0 : e1);
    WG_BARRIER();
    if (w == 15) {
      float p[16];
#pragma unroll
      for (int i = 0; i < 16; ++i) p[i] = L.red[i][lane];
      const float v = SUM16(p);
      const u32 pr = pkbf(v, dpp<XOR1>(v)), pr2 = asu(dpp<XOR2>(asf(pr)));
      __builtin_amdgcn_raw_buffer_store_b128(u32x4{pr, tag, pr2, tag}, rws, (lane & 3) ? WS_END : WS_QG + head * 512 + qh * 256 + lane * 4, 0, AUX_SC1);
    }
    TS(2);
    WG_BARRIER();
  }
  if (w == 0) {
    const int go = lane < 32 ? WS_QG + head * 512 + lane * 16 : WS_END;
    u32x4 g = __builtin_amdgcn_raw_buffer_load_b128(rws, go, 0, AUX_SC1);
    __builtin_amdgcn_s_sleep(8);
    _Pragma("nounroll") for (u32 spin = 0; spin < SPIN_CAP; ++spin) {
      const u32x4 g2 = __builtin_amdgcn_raw_buffer_load_b128(rws, go, 0, AUX_SC1);
      asm volatile("" ::: "memory");
      if ((u32)__ballot(gran_ok(g, tag)) == 0xffffffffu) { if (lane < 32) *(u32x2*)&L.qs[2 * lane] = u32x2{g[0], g[2]}; break; }
      __builtin_amdgcn_s_sleep(2);
      g = __builtin_amdgcn_raw_buffer_load_b128(rws, go, 0, AUX_SC1);
      asm volatile("" ::: "memory");
      if ((u32)__ballot(gran_ok(g2, tag)) == 0xffffffffu) { if (lane < 32) *(u32x2*)&L.qs[2 * lane] = u32x2{g2[0], g2[2]}; break; }
      __builtin_amdgcn_s_sleep(2);
    }
    asm volatile("s_waitcnt lgkmcnt(0)" ::: "memory");
    if (lane == 0) L.qflag = 1;
    TS(3);
  }
  bool paced = w != 0;
  const u32 tl_addr = (u32)(size_t)(LAS u32x4*)&L.tile[w][0];
  DMA4(rk, tl_addr, (t0 + w) * 4096);
  if (w == 15) {
    asm volatile("s_waitcnt vmcnt(4)" ::: "memory");
    if (lane == 0) __hip_atomic_store(RDY + slot, E, __ATOMIC_RELAXED, __HIP_MEMORY_SCOPE_AGENT);
  }
  DMA4(rv, tl_addr + 4096, (t0 + w) * 4096);
  const u32x4 wov = WOLD((const u32x4*)(wo + (size_t)(grp * 512 + 32 * w + (lane >> 1)) * D + 16 * cs + 8 * (lane & 1)));
  const u16* ap_ = tid < 172 ? w1 + tid * 8 : tid < 344 ? b1 + (tid - 172) * 8 : tid < 516 ? w2 + (tid - 344) * 8 : x + (tid < 580 ? tid - 516 : 0) * 8;
  const u32x4 auxv = *(const u32x4*)ap_;
  const u32 b2raw = __builtin_amdgcn_raw_buffer_load_b16(__builtin_amdgcn_make_buffer_rsrc((void*)b2, 0, 2, 0x00020000), 0, 0, 0);
  u32x4 rd0, rd1;
#define SAMPLE_RDY() do { rd0 = __builtin_amdgcn_raw_buffer_load_b128(rws, w == 4 ? WS_RDY + lane * 16 : WS_END, 0, AUX_SC1); \
    rd1 = __builtin_amdgcn_raw_buffer_load_b128(rws, w == 4 ? WS_RDY + 1024 + lane * 16 : WS_END, 0, AUX_SC1); } while (0)
#define RDY_OK() __all(((rd0[0] ^ elo) | (rd0[2] ^ elo) | (rd1[0] ^ elo) | (rd1[2] ^ elo) | (rd0[1] ^ ehi) | (rd0[3] ^ ehi) | (rd1[1] ^ ehi) | (rd1[3] ^ ehi)) == 0u)
  const u32 elo = (u32)E, ehi = (u32)(E >> 32);
  SAMPLE_RDY();
  asm volatile("" ::: "memory");
  LDP(kA, rk, t0 + 16 + w); LDP(vA, rv, t0 + 16 + w); LDP(kB, rk, t0 + 32 + w); LDP(vB, rv, t0 + 32 + w);
  WG_BARRIER();
  if (w != 0) TS(3);
  const u32x4 qp = *(const u32x4*)&L.qs[4 * c];

  float m = -INFINITY, l = 0.f;
  f32x2 acc[4] = {{0.f, 0.f}, {0.f, 0.f}, {0.f, 0.f}, {0.f, 0.f}};
  const bool c8 = c & 8, c4 = c & 4;
  const u32 ctr_addr = (u32)(size_t)(LAS u32*)&L.ctr, two = 2u;
#define GRAB(dst) do { u64 sv_; asm volatile("s_mov_b64 %1, exec\n\ts_mov_b64 exec, 1\n\tds_add_rtn_u32 %0, %2, %3\n\ts_mov_b64 exec, %1" \
    : "=&v"(dst), "=&s"(sv_) : "v"(ctr_addr), "v"(two) : "memory"); } while (0)
#define GRAB_WAIT(dst) asm volatile("s_waitcnt lgkmcnt(0)" : "+v"(dst) :: "memory")
#define TICKET(want) (u32)__builtin_amdgcn_raw_ptr_buffer_atomic_add_i32(2, rws, ((want) && lane == 0) ? WS_PC + head * 128 : WS_END, 0, 0)
#define PIN() do { asm volatile("" ::: "memory"); __builtin_amdgcn_sched_barrier(0); } while (0)
#define QK(xx, kk) do { \
    _Pragma("unroll") for (int t_ = 0; t_ < 4; ++t_) { float d_ = dot2(kk[t_][0], qp[0], 0.f); d_ = dot2(kk[t_][1], qp[1], d_); d_ = dot2(kk[t_][2], qp[2], d_); xx[t_] = dot2(kk[t_][3], qp[3], d_); } \
    asm volatile("" : "+v"(xx[0]), "+v"(xx[1]), "+v"(xx[2]), "+v"(xx[3])); } while (0)
#define SMPV(x_, vv) do { \
    const float y0_ = (c8 ? x_[2] : x_[0]) + dpp<ROR8>(c8 ? x_[0] : x_[2]); \
    const float y1_ = (c8 ? x_[3] : x_[1]) + dpp<ROR8>(c8 ? x_[1] : x_[3]); \
    float z_ = (c4 ? y1_ : y0_) + dpp<HMIR>(c4 ? y0_ : y1_); \
    z_ += dpp<XOR2>(z_); z_ += dpp<XOR1>(z_); \
    const float s_ = rbf(rbf(z_) * SCALE); \
    float mx_ = fmaxf(s_, dpp<HMIR>(s_)); mx_ = fmaxf(mx_, dpp<ROR8>(mx_)); \
    const float mn_ = fmaxf(m, mx_); \
    const float al_ = ex2((m - mn_) * LOG2E), p_ = ex2((s_ - mn_) * LOG2E); \
    m = mn_; l = fmaf(l, al_, p_); \
    _Pragma("unroll") for (int j_ = 0; j_ < 4; ++j_) acc[j_] *= al_; \
    { const float p0_ = dpp<NBC + 0>(p_), p1_ = dpp<NBC + 4>(p_), p2_ = dpp<NBC + 8>(p_), p3_ = dpp<NBC + 12>(p_); \
      _Pragma("unroll") for (int j_ = 0; j_ < 4; ++j_) { \
        acc[j_] += f32x2{bflo(vv[0][j_]), bfhi(vv[0][j_])} * p0_; acc[j_] += f32x2{bflo(vv[1][j_]), bfhi(vv[1][j_])} * p1_; \
        acc[j_] += f32x2{bflo(vv[2][j_]), bfhi(vv[2][j_])} * p2_; acc[j_] += f32x2{bflo(vv[3][j_]), bfhi(vv[3][j_])} * p3_; } } \
    asm volatile("" : "+v"(acc[0]), "+v"(acc[1]), "+v"(acc[2]), "+v"(acc[3]), "+v"(l), "+v"(m)); } while (0)
  float xa_[4], xb_[4];
  QK(xa_, kA); PIN();
#pragma unroll
  for (int i = 0; i < 4; ++i) kA[i] = L.tile[w][i * 64 + lane];
  PIN();
  SMPV(xa_, vA); PIN();
#pragma unroll
  for (int i = 0; i < 4; ++i) vA[i] = L.tile[w][256 + i * 64 + lane];
  PIN();
  {
    u16* wf = (u16*)&L.tile[w][(lane >> 4) * 16 + (lane & 1) * 8] + ((lane >> 1) & 7);
#pragma unroll
    for (int j = 0; j < 4; ++j) { wf[16 * j] = (u16)(wov[j] & 0xffffu); wf[16 * j + 8] = (u16)(wov[j] >> 16); }
    L.tile[w][64 + lane] = auxv;
    if (tid == 0) L.b2 = b2raw;
    if (RDY_OK() && lane == 0) L.rdyflag = 1;
    PIN();
  }
  TS(11);
  if (w < NSW) {
    bool have = false, pending = false;
    u32 tk = 0, tkval = 0;
    for (int it = 0; it < 1024; ++it) {
      int tv;
      GRAB(tv);
      QK(xa_, kA); QK(xb_, kB);
      __builtin_amdgcn_sched_barrier(0);
      GRAB_WAIT(tv);
      int tn = __builtin_amdgcn_readfirstlane(tv);
      if (pending) { tkval = __builtin_amdgcn_readfirstlane(tk); have = true; }
      if (tn < tend) pending = !have && tn + LEAD >= tend;
      else {
        if (!have) tkval = __builtin_amdgcn_readfirstlane(TICKET(true));
        tn = min(POOL0 + (int)tkval, 1024); have = false; pending = tn < 1024;
      }
      LDT(kA, rk, tn); LDT(kB, rk, tn + 1); tk = TICKET(pending);
      PIN();
      SMPV(xa_, vA); PIN();
      LDT(vA, rv, tn); PIN();
      SMPV(xb_, vB); PIN();
      LDT(vB, rv, tn + 1); PIN();
      if (tn >= 1024) break;
    }
  } else {
    QK(xa_, kA); QK(xb_, kB); PIN();
    SMPV(xa_, vA); PIN();
    SMPV(xb_, vB); PIN();
  }
  TS(4);

  {
    float lr = l + dpp<HMIR>(l); lr += dpp<ROR8>(lr);
    const float Mw = xrow_max(m), sc = ex2((m - Mw) * LOG2E);
    lr = xrow_sum(lr * sc);
#pragma unroll
    for (int j = 0; j < 4; ++j) acc[j] *= sc;
    L.red[w][8 * c + r] = rows4(acc[0][0], acc[0][1], acc[1][0], acc[1][1]);
    L.red[w][8 * c + 4 + r] = rows4(acc[2][0], acc[2][1], acc[3][0], acc[3][1]);
    if (lane == 0) L.ml[w] = f32x2{Mw, lr};
  }
  WG_BARRIER();
  TS(5);
#define LDS_WAIT(word, n) do { _Pragma("nounroll") for (u32 spin_ = 0; spin_ < (1u << 22); ++spin_) \
    if (__hip_atomic_load(&(word), __ATOMIC_RELAXED, __HIP_MEMORY_SCOPE_WORKGROUP) >= (n)) break; \
    asm volatile("" ::: "memory"); } while (0)
#define LDS_SIGNAL(word) do { asm volatile("s_waitcnt lgkmcnt(0)" ::: "memory"); \
    if (lane == 0) __hip_atomic_fetch_add(&(word), 1u, __ATOMIC_RELAXED, __HIP_MEMORY_SCOPE_WORKGROUP); } while (0)
  const int dd = (tid - 512) & 511;
  if (w < 4) {
    if (w == 0) {
      const f32x2 mlv = L.ml[c];
      f32x2 pv[16];
#pragma unroll
      for (int i = 0; i < 16; ++i) pv[i] = *(const f32x2*)&L.red[i][2 * lane];
      float M = fmaxf(mlv[0], dpp<XOR1>(mlv[0])); M = fmaxf(M, dpp<XOR2>(M)); M = fmaxf(M, dpp<HMIR>(M)); M = fmaxf(M, dpp<ROR8>(M));
      const float wt = ex2((mlv[0] - M) * LOG2E);
      float ls = sum8(mlv[1] * wt); ls += dpp<ROR8>(ls);
      f32x2 o0 = pv[0] * dpp<NBC + 0>(wt), o1 = pv[1] * dpp<NBC + 1>(wt), o2 = pv[2] * dpp<NBC + 2>(wt), o3 = pv[3] * dpp<NBC + 3>(wt);
#define MRG(i) o0 += pv[i] * dpp<NBC + i>(wt); o1 += pv[i + 1] * dpp<NBC + i + 1>(wt); o2 += pv[i + 2] * dpp<NBC + i + 2>(wt); o3 += pv[i + 3] * dpp<NBC + i + 3>(wt);
      MRG(4) MRG(8) MRG(12)
      const f32x2 o = (o0 + o1) + (o2 + o3);
      const u32 hi = nosent(pkbf(o[0], o[1])), lo = nosent(pkbf(o[0] - bflo(hi), o[1] - bfhi(hi)));
      const float sel = asf((lane & 1) ? hi : lo), own = asf((lane & 1) ? lo : hi);
      const float nb = dpp<XOR1>(sel);
      const float e0 = (lane & 1) ? nb : own, e1 = (lane & 1) ? own : nb;
      const u32x4 pc = {asu(e0), asu(e1), asu(dpp<XOR2>(e0)), asu(dpp<XOR2>(e1))};
      __builtin_amdgcn_raw_buffer_store_b128(pc, rws, (lane & 2) ? WS_END : WS_AS + slot * 512 + (lane & 1) * 256 + (lane >> 2) * 16, 0, AUX_SC1);
      __builtin_amdgcn_raw_buffer_store_b64(u32x2{nosent(asu(M)), nosent(asu(ls))}, rws, lane == 0 ? WS_ML + slot * 8 : WS_END, 0, AUX_SC1);
      TS(10);
    }
    u32x4 bfr[4];
#pragma unroll
    for (int ks = 0; ks < 4; ++ks) bfr[ks] = L.tile[4 * w + ks][lane];
    LDS_WAIT(L.rdyflag, 1u);
    const int ps = (grp * 4 + w) * 8 + (lane & 7);
    const int ao = WS_AS + ps * 512 + (lane & 8) * 32 + r * 16;
    u32x4 af[4];
    u32x2 mlp;
    bool ok = false;
    _Pragma("nounroll") for (u32 spin = 0; spin < SPIN_CAP; ++spin) {
      if (!ok) {
#pragma unroll
        for (int ks = 0; ks < 4; ++ks) af[ks] = __builtin_amdgcn_raw_buffer_load_b128(rws, ao + 64 * ks, 0, AUX_SC1);
        mlp = __builtin_amdgcn_raw_buffer_load_b64(rws, WS_ML + ps * 8, 0, AUX_SC1);
        ok = ok4(af[0]) && ok4(af[1]) && ok4(af[2]) && ok4(af[3]) && max(mlp[0], mlp[1]) != SENT;
      }
      const u64 miss = __ballot(!ok);
      if (!miss) break;
      if (__builtin_popcountll(miss) > 16) __builtin_amdgcn_s_sleep(24); else __builtin_amdgcn_s_sleep(2);
    }
    TS(6);
    f32x4 d4 = {0.f, 0.f, 0.f, 0.f};
#pragma unroll
    for (int ks = 0; ks < 4; ++ks) d4 = __builtin_amdgcn_mfma_f32_16x16x32_bf16(__builtin_bit_cast(bf16x8, af[ks]), __builtin_bit_cast(bf16x8, bfr[ks]), d4, 0, 0, 0);
    const float Ms = asf(mlp[0]);
    float Mx = fmaxf(Ms, dpp<XOR1>(Ms)); Mx = fmaxf(Mx, dpp<XOR2>(Mx)); Mx = fmaxf(Mx, dpp<HMIR>(Mx));
    const float wt = ex2((Ms - Mx) * LOG2E);
    const float cf = wt * rcp(sum8(wt * asf(mlp[1])));
    const float cs4 = (r & 1) ? dpp<HMIR>(cf) : cf;
    const bool od = r & 1;
    const float zp = xrow_sum(fmaf(od ? dpp<NBC + 3>(cs4) : dpp<NBC + 0>(cs4), d4[0], (od ? dpp<NBC + 2>(cs4) : dpp<NBC + 1>(cs4)) * d4[1]) +
                              fmaf(od ? dpp<NBC + 1>(cs4) : dpp<NBC + 2>(cs4), d4[2], (od ? dpp<NBC + 0>(cs4) : dpp<NBC + 3>(cs4)) * d4[3]));
    if (lane < 16) L.zpart[w][lane] = zp;
    LDS_SIGNAL(L.zcnt);
    if (w == 0) {
      LDS_WAIT(L.zcnt, 4u);
      const f32x4 z = (*(const f32x4*)&L.zpart[0][4 * (lane & 3)] + *(const f32x4*)&L.zpart[1][4 * (lane & 3)]) +
                      (*(const f32x4*)&L.zpart[2][4 * (lane & 3)] + *(const f32x4*)&L.zpart[3][4 * (lane & 3)]);
      const u32x4 zz = {nosent(asu(z[0])), nosent(asu(z[1])), nosent(asu(z[2])), nosent(asu(z[3]))};
      __builtin_amdgcn_raw_buffer_store_b128(zz, rws, lane < 4 ? WS_ZS + grp * 2048 + cs * 64 + lane * 16 : WS_END, 0, AUX_SC1);
    }
    TS(7);
  } else if (w == 4) {
    if (!__builtin_amdgcn_readfirstlane(L.rdyflag)) {
      _Pragma("nounroll") for (u32 spin = 0; spin < SPIN_CAP; ++spin) { SAMPLE_RDY(); if (RDY_OK()) break; }
      if (lane == 0) L.rdyflag = 1;
    }
    u32x4 mv[4];
    _Pragma("nounroll") for (u32 spin = 0; spin < SPIN_CAP; ++spin) {
#pragma unroll
      for (int i = 0; i < 4; ++i) mv[i] = __builtin_amdgcn_raw_buffer_load_b128(rws, WS_MS + i * 1024 + lane * 16, 0, AUX_SC1);
      if (__all(ok4(mv[0]) && ok4(mv[1]) && ok4(mv[2]) && ok4(mv[3]))) break;
      __builtin_amdgcn_s_sleep(8);
    }
#pragma unroll
    for (int i = 0; i < 4; ++i) L.ms[i * 64 + lane] = mv[i];
    LDS_SIGNAL(L.gcnt);
  }
#define AUX32(i) (((const u32*)&L.tile[(i) >> 8][64])[(i) & 255])
#define AUX16(i) (((const u16*)&L.tile[(i) >> 9][64])[(i) & 511])
  float x1 = 0.f, xsb = 0.f, xcb = 0.f;
  if (w >= 8) {
    LDS_WAIT(L.rdyflag, 1u);
    const int zo = WS_ZS + (w - 8) * 2048 + lane * 16;
    u32x4 z0, z1;
    bool ok0 = false, ok1 = false;
    _Pragma("nounroll") for (u32 spin = 0; spin < SPIN_CAP; ++spin) {
      if (!ok0) z0 = __builtin_amdgcn_raw_buffer_load_b128(rws, zo, 0, AUX_SC1);
      if (!ok1) z1 = __builtin_amdgcn_raw_buffer_load_b128(rws, zo + 1024, 0, AUX_SC1);
      ok0 = ok4(z0); ok1 = ok4(z1);
      const u64 miss = __ballot(!(ok0 && ok1));
      if (!miss) break;
      if (__builtin_popcountll(miss) > 48) __builtin_amdgcn_s_sleep(16); else __builtin_amdgcn_s_sleep(2);
    }
    L.zs[w - 8][lane] = __builtin_bit_cast(f32x4, z0);
    L.zs[w - 8][64 + lane] = __builtin_bit_cast(f32x4, z1);
    LDS_SIGNAL(L.gcnt);
    const float xv = bf2f(AUX16(3 * F + dd));
    LDS_WAIT(L.gcnt, 9u);
    TS(6);
    const float* zf = (const float*)L.zs + dd;
    const u16* msv = (const u16*)L.ms + dd;
    const float at = ((zf[0] + zf[512]) + (zf[1024] + zf[1536])) + ((zf[2048] + zf[2560]) + (zf[3072] + zf[3584]));
    x1 = rbf(xv + rbf(bf2f(msv[0]) * rbf(at)));
    xsb = bf2f(msv[512]); xcb = rbf(1.f + bf2f(msv[1024]));
    const f32x2 p = ln_part(x1);
    if (lane == 0) L.st1[w - 8] = p;
  }
  const int fp = min(tid, F / 2 - 1);
  const u32 pw1 = AUX32(fp), pb1 = AUX32(F / 2 + fp), pw2 = tid < F / 2 ? AUX32(F + fp) : 0u;
  WG_BARRIER();
  TS(8);
  if (w >= 8) {
    float mb, rden;
    ln_comb(L.st1[lane & 7], mb, rden);
    const float hh = rbf(rbf(ln_el(x1, mb, rden) * xcb) + xsb);
    if ((dd >> 1) == bid) { L.x1d[dd & 1] = x1; L.hd[dd & 1] = hh; }
    const f32x2 p = ln_part(hh);
    if (lane == 0) L.st2[w - 8] = p;
  }
  WG_BARRIER();
  TS(9);
  if (w < 11) {
    float mb, rden;
    ln_comb(L.st2[lane & 7], mb, rden);
    const f32x2 w1f = {bflo(pw1), bfhi(pw1)}, b1f = {bflo(pb1), bfhi(pb1)};
    f32x2 y;
#pragma unroll
    for (int k = 0; k < 2; ++k) {
      const float t = ln_el(L.hd[k], mb, rden);
      const f32x2 tw = w1f * t;
      const u32 twb = pkbf(tw[0], tw[1]);
      const f32x2 us = f32x2{bflo(twb), bfhi(twb)} + b1f;
      const u32 ub = pkbf(us[0], us[1]);
      const f32x2 uu = {bflo(ub), bfhi(ub)};
      constexpr float KK = -2.f * 0.7978845608028654f * LOG2E;
      const f32x2 zz = uu * (uu * uu * (0.044715f * KK) + KK);
      const f32x2 sg = {rcp(1.f + ex2(zz[0])), rcp(1.f + ex2(zz[1]))};
      const f32x2 gg = uu * sg;
      y[k] = wave_sum(dot2(pkbf(gg[0], gg[1]), pw2, 0.f));
    }
    if (lane == 0) L.yred[w] = y;
  }
  WG_BARRIER();
  if (w == 0) {
    const int k = r & 1;
    float y = c < 11 ? L.yred[c][k] : 0.f;
    y = sum8(y); y += dpp<ROR8>(y);
    if (lane == 0 || lane == 16) {
      const float yy = rbf(rbf(y) + asf(L.b2 << 16));
      out[2 * bid + k] = (u16)(pkbf(L.x1d[k] + rbf(bf2f(((const u16*)L.ms)[1536 + 2 * bid + k]) * yy), 0.f) & 0xffffu);
    }
    if (bid == 0 && lane == 0) __hip_atomic_store((gu64*)(ws + WS_E), E, __ATOMIC_RELAXED, __HIP_MEMORY_SCOPE_AGENT);
  }
}

extern "C" void kernel_launch(void* const* d_in, const int* in_sizes, int n_in, void* d_out, int out_size, void* d_ws, size_t ws_size, hipStream_t stream) {
  fused<<<256, 1024, 0, stream>>>((const u16*)d_in[0], (const u16*)d_in[1], (const u16*)d_in[2], (const u16*)d_in[3], (const u16*)d_in[4],
                                  (const u16*)d_in[6], (const u16*)d_in[7], (const u16*)d_in[8], (const u16*)d_in[9], (const u16*)d_in[10],
                                  (const u16*)d_in[11], (u16*)d_out, (unsigned char*)d_ws);
}
```

```cpp
#include <hip/hip_runtime.h>
#include <hip/hip_bf16.h>
#include <stdint.h>

typedef unsigned u32;
typedef unsigned short u16;
typedef unsigned long long u64;
typedef u32 u32x4 __attribute__((ext_vector_type(4)));
typedef u32 u32x2 __attribute__((ext_vector_type(2)));
typedef float f32x4 __attribute__((ext_vector_type(4)));
typedef float f32x2 __attribute__((ext_vector_type(2)));
typedef __bf16 bf16x2 __attribute__((ext_vector_type(2)));
typedef __bf16 bf16x8 __attribute__((ext_vector_type(8)));
#define GAS __attribute__((address_space(1)))
#define CAS __attribute__((address_space(4)))
#define LAS __attribute__((address_space(3)))
typedef GAS u64 gu64;

#define PACE 6
#define NLM 128
#define NLQ 112
#define NEARLY 16
#define NEARLYM 16
#define GSLEEP 12
#define SWP 0
#define PACEB 6
#define AUXWO 0

constexpr int D = 512, H = 32, SK = 16384, DH = 128, F = 1376;
constexpr float LOG2E = 1.4426950408889634f;
constexpr float SCALE = 0.08838834764831845f;
constexpr u64 EPOCH_STEP = 0x9E3779B97F4A7C15ull;
constexpr u32 SPIN_CAP = 1u << 13;
constexpr int AUX_NT = 2, AUX_SC1 = 16;
constexpr u32 SENT = 0xFFFFFFFFu;

constexpr int NLR = (1024 - NLM - 2 * NLQ) / 5, NLL = 1024 - NLM - 2 * NLQ - 4 * NLR;
static_assert(NLQ >= 48 && NLM >= 48 && NLR >= 48, "three prefetched tiles per wave");
__device__ __forceinline__ int tile_start(int rho) {
  return rho * NLR - (rho >= 1 ? NLR - NLM : 0) - (rho >= 2 ? NLR - NLQ : 0) - (rho >= 4 ? NLR - NLQ : 0);
}
__device__ __forceinline__ int tile_count(int rho) { return rho == 0 ? NLM : (rho == 1 || rho == 3) ? NLQ : rho == 7 ? NLL : NLR; }

constexpr int WS_E = 0;
constexpr int WS_RDY = 4096;
constexpr int WS_MG = 8192;
constexpr int WS_MS = 16384;
constexpr int WS_QG = 32768;
constexpr int WS_ZS = 65536;
constexpr int WS_AS = 262144;
constexpr int WS_END = WS_AS + 256 * 1024;

__device__ __forceinline__ float asf(u32 u) { return __builtin_bit_cast(float, u); }
__device__ __forceinline__ u32 asu(float f) { return __builtin_bit_cast(u32, f); }
__device__ __forceinline__ float bflo(u32 u) { return asf(u << 16); }
__device__ __forceinline__ float bfhi(u32 u) { return asf(u & 0xffff0000u); }
__device__ __forceinline__ float bf2f(u16 b) { return asf((u32)b << 16); }
__device__ __forceinline__ u32 pkbf(float lo, float hi) { return __builtin_bit_cast(u32, bf16x2{(__bf16)lo, (__bf16)hi}); }
__device__ __forceinline__ float rbf(float x) { return asf(pkbf(0.f, x)); }
__device__ __forceinline__ float dot2(u32 k, u32 q, float acc) {
  return __builtin_amdgcn_fdot2_f32_bf16(__builtin_bit_cast(bf16x2, k), __builtin_bit_cast(bf16x2, q), acc, false);
}
template <int CTRL> __device__ __forceinline__ float dpp(float x) {
  return asf((u32)__builtin_amdgcn_mov_dpp((int)asu(x), CTRL, 0xf, 0xf, true));
}
template <int CA, int CB> __device__ __forceinline__ float dpp_evod(float x) {
  const int t = __builtin_amdgcn_update_dpp(0, (int)asu(x), CA, 0x5, 0xf, true);
  return asf((u32)__builtin_amdgcn_update_dpp(t, (int)asu(x), CB, 0xA, 0xf, true));
}
__device__ __forceinline__ float fmax2(float a, float b) { float d; asm("v_max_f32 %0, %1, %2\n\ts_nop 1" : "=v"(d) : "v"(a), "v"(b)); return d; }
constexpr int XOR1 = 0xB1, XOR2 = 0x4E, HMIR = 0x141, ROR4 = 0x124, ROR8 = 0x128, NBC = 0x150;
__device__ __forceinline__ float xrow_max(float x) {
  auto s = __builtin_amdgcn_permlane16_swap(asu(x), asu(x), false, false);
  x = fmax2(asf(s[0]), asf(s[1]));
  auto t = __builtin_amdgcn_permlane32_swap(asu(x), asu(x), false, false);
  return fmax2(asf(t[0]), asf(t[1]));
}
__device__ __forceinline__ float tr16(float a, float b) {
  auto s = __builtin_amdgcn_permlane16_swap(asu(a), asu(b), false, false);
  return asf(s[0]) + asf(s[1]);
}
__device__ __forceinline__ float tr32(float a, float b) {
  auto s = __builtin_amdgcn_permlane32_swap(asu(a), asu(b), false, false);
  return asf(s[0]) + asf(s[1]);
}
__device__ __forceinline__ float xrow_sum(float x) { x = tr16(x, x); return tr32(x, x); }
__device__ __forceinline__ float rows4(float a0, float a1, float a2, float a3) { return tr32(tr16(a0, a1), tr16(a2, a3)); }
__device__ __forceinline__ float red8(float a0, float a1, float a2, float a3, float a4, float a5, float a6, float a7, bool b3) {
  const float e0 = rows4(a0, a1, a2, a3), e1 = rows4(a4, a5, a6, a7);
  return (b3 ? e1 : e0) + dpp<ROR8>(b3 ? e0 : e1);
}
__device__ __forceinline__ float sum8(float x) { x += dpp<XOR1>(x); x += dpp<XOR2>(x); return x + dpp<HMIR>(x); }
__device__ __forceinline__ float wave_sum(float x) { x = sum8(x); x += dpp<ROR8>(x); return xrow_sum(x); }
__device__ __forceinline__ float ex2(float x) { return __builtin_amdgcn_exp2f(x); }
__device__ __forceinline__ float rcp(float x) { return __builtin_amdgcn_rcpf(x); }

__device__ __forceinline__ bool gran_ok(u32x4 g, u32 tag) { return g[1] == tag && g[3] == tag; }
__device__ __forceinline__ u32 nosent(u32 v) { return min(v, SENT - 1u); }
__device__ __forceinline__ u32 max4(u32x4 v) { return max(max(v[0], v[1]), max(v[2], v[3])); }

__device__ __forceinline__ float ln_el(float x, float mb, float rden) { return rbf(rbf(x - mb) * rden); }
__device__ __forceinline__ float ln_rden(float var) { return rcp(rbf(__builtin_amdgcn_sqrtf(rbf(rbf(var) + 1e-5f)))); }
__device__ __forceinline__ float wave_sum2(float a, float b, bool odd) {
  float t = (odd ? b : a) + dpp<XOR1>(odd ? a : b);
  t += dpp<XOR2>(t); t += dpp<ROR4>(t); t += dpp<ROR8>(t);
  return xrow_sum(t);
}
__device__ __forceinline__ f32x2 ln_part(float v, bool odd) {
  const float p = asf(__builtin_amdgcn_readfirstlane(asu(v))), d = v - p;
  const float t = wave_sum2(d, d * d, odd);
  const float s1 = asf(__builtin_amdgcn_readlane(asu(t), 0)), s2 = asf(__builtin_amdgcn_readlane(asu(t), 1));
  return f32x2{fmaf(s1, 1.f / 64.f, p), fmaf(s1 * s1, -1.f / 64.f, s2)};
}
__device__ __forceinline__ void ln_comb(f32x2 p, float& mb, float& rden) {
  const float p0 = asf(__builtin_amdgcn_readfirstlane(asu(p[0]))), d = p[0] - p0;
  const float s1 = sum8(d), s2 = sum8(fmaf(d * d, 64.f, p[1]));
  mb = rbf(fmaf(s1, 0.125f, p0));
  rden = ln_rden(fmaxf(fmaf(s1 * s1, -8.f, s2), 0.f) * (1.f / 512.f));
}

struct Lds {
  u32x4 tile[16][512];
  float red[16][128];
  u32x4 ms[256];
  u32x2 hbuf[16][8];
  f32x2 run[8][64], runml[8];
  f32x2 st1[8], st2[8], yred[16];
  float zpart[4][16];
  float x1d[2], hd[2];
  u32 qs[64];
  u32 msflag, zcnt, zflag, qflag, rdyflag, fin, chain[4];
};

#define TS(k) do {} while (0)

__global__ void __launch_bounds__(1024, 4)
fused(const u16* __restrict__ x, const u16* __restrict__ Kc, const u16* __restrict__ Vc, const u16* __restrict__ wq, const u16* __restrict__ wo,
      const u16* __restrict__ w1, const u16* __restrict__ b1, const u16* __restrict__ w2, const u16* __restrict__ b2, const u16* __restrict__ cc,
      const u16* __restrict__ wmod, u16* __restrict__ out, unsigned char* ws) {
  __shared__ Lds L;
  TS(0);
  asm volatile("" :: "s"(x), "s"(Kc), "s"(Vc), "s"(wq), "s"(wo), "s"(w1), "s"(b1), "s"(w2), "s"(b2), "s"(cc), "s"(wmod), "s"(out), "s"(ws));
  const int tid = threadIdx.x, lane = tid & 63, w = __builtin_amdgcn_readfirstlane(tid >> 6);
  const int r = lane >> 4, c = lane & 15;
  const bool b3 = lane & 8;
  const int bid = blockIdx.x, rho = bid >> 5, u = bid & 31, head = (u & 24) | ((u + rho) & 7);
  const int slot = head * 8 + rho;
  const int grp = bid & 7, cs = bid >> 3;
  const __amdgpu_buffer_rsrc_t rws = __builtin_amdgcn_make_buffer_rsrc((void*)ws, 0, WS_END, 0x00020000);
#define LD_WS(off) __builtin_amdgcn_raw_buffer_load_b128(rws, off, 0, AUX_SC1)
#define ST_WS(v, off) __builtin_amdgcn_raw_buffer_store_b128(v, rws, off, 0, AUX_SC1)

  const int ntile = tile_count(rho);
  const int sw = !SWP ? w : (w & 3) ? (w >> 2) * 3 + (w & 3) - 1 : 12 + (((w >> 2) + 3) & 3);
  const size_t kvbase = ((size_t)head * SK + (size_t)tile_start(rho) * 16) * DH;
  const __amdgpu_buffer_rsrc_t rk = __builtin_amdgcn_make_buffer_rsrc((void*)(Kc + kvbase), 0, ntile * 4096, 0x00020000);
  const __amdgpu_buffer_rsrc_t rv = __builtin_amdgcn_make_buffer_rsrc((void*)(Vc + kvbase), 0, ntile * 4096, 0x00020000);
  const int voff = lane * 16;
  u32x4 kA[4], vA[4], kB[4], vB[4];
#define LDT(dst, rs, t) do { _Pragma("unroll") for (int i_ = 0; i_ < 4; ++i_) dst[i_] = __builtin_amdgcn_raw_buffer_load_b128(rs, voff + i_ * 1024, (t) * 4096, AUX_NT); } while (0)
#define PACE_WAIT(P) do { if (paced) { paced = !__builtin_amdgcn_readfirstlane(__hip_atomic_load(&L.qflag, __ATOMIC_RELAXED, __HIP_MEMORY_SCOPE_WORKGROUP)); \
      if (paced) asm volatile("s_waitcnt vmcnt(%0)" :: "n"((P) - 1) : "memory"); } } while (0)
#define DMA1(rs, ldsb, so, OFS) do { PACE_WAIT(PACE); asm volatile("s_mov_b32 m0, %0\n\ts_nop 0\n\tbuffer_load_dwordx4 %1, %2, %3 offen offset:" #OFS " nt lds" \
      :: "s"(ldsb), "v"(voff), "s"(rs), "s"(so) : "memory"); } while (0)
#define DMA4(rs, ldsb, so) do { DMA1(rs, ldsb, so, 0); DMA1(rs, ldsb, so, 1024); DMA1(rs, ldsb, so, 2048); DMA1(rs, ldsb, so, 3072); } while (0)
#define LDP(dst, rs, t, P) do { _Pragma("unroll") for (int i_ = 0; i_ < 4; ++i_) { PACE_WAIT(P); \
    dst[i_] = __builtin_amdgcn_raw_buffer_load_b128(rs, voff + i_ * 1024, (t) * 4096, AUX_NT); asm volatile("" ::: "memory"); } } while (0)
#define WG_BARRIER() asm volatile("s_waitcnt lgkmcnt(0)\n\ts_barrier" ::: "memory")
#define LDS_WAIT(word, n) do { _Pragma("nounroll") for (u32 spin_ = 0; spin_ < (1u << 22); ++spin_) \
    if (__hip_atomic_load(&(word), __ATOMIC_RELAXED, __HIP_MEMORY_SCOPE_WORKGROUP) >= (n)) break; \
    asm volatile("" ::: "memory"); } while (0)
#define LDS_SIGNAL(word) asm volatile("s_mov_b64 exec, 1\n\tds_add_u32 %0, %1\n\ts_mov_b64 exec, -1" :: "v"((u32)(size_t)(LAS u32*)&(word)), "v"(1u) : "memory")
#define LDS_TICKET(dst, word) asm volatile("s_mov_b64 exec, 1\n\tds_add_rtn_u32 %0, %1, %2\n\ts_mov_b64 exec, -1" : "=&v"(dst) : "v"((u32)(size_t)(LAS u32*)&(word)), "v"(1u) : "memory")
#define LGKM0(x) asm volatile("s_waitcnt lgkmcnt(0)" : "+v"(x) :: "memory")
#define SUM16(p) ((((p[0] + p[1]) + (p[2] + p[3])) + ((p[4] + p[5]) + (p[6] + p[7]))) + (((p[8] + p[9]) + (p[10] + p[11])) + ((p[12] + p[13]) + (p[14] + p[15]))))

  const bool qrole = rho == 1 || rho == 3, mrole = rho == 0 || qrole;
  const int unit = ((rho + 1) >> 1) * 32 + u;
  const int qh = rho >> 1;
  u32x4 wqv[4], wm0, wm1;
  u32 c0r, c1r;
#define ASM_LD4(dst, vo, rs, so) asm volatile("buffer_load_dwordx4 %0, %1, %2, %3 offen nt" : "=v"(dst) : "v"(vo), "s"(rs), "s"(so) : "memory")
  if (mrole) {
    const __amdgpu_buffer_rsrc_t rm = __builtin_amdgcn_make_buffer_rsrc((void*)wmod, 0, D * 6 * D * 2, 0x00020000);
    const __amdgpu_buffer_rsrc_t rc = __builtin_amdgcn_make_buffer_rsrc((void*)cc, 0, D * 2, 0x00020000);
    const int mo = (32 * w + (lane >> 2)) * (6 * D * 2) + unit * 64 + (lane & 3) * 16, co = (32 * w + (lane >> 2)) * 2;
    asm volatile("s_nop 4\n\tbuffer_load_ushort %0, %2, %3, 0 offen\n\tbuffer_load_ushort %1, %2, %3, 0 offen offset:32" : "=&v"(c0r), "=&v"(c1r) : "v"(co), "s"(rc) : "memory");
    ASM_LD4(wm0, mo, rm, 0); ASM_LD4(wm1, mo, rm, 16 * (6 * D * 2));
  }
  if (qrole) {
    const __amdgpu_buffer_rsrc_t rq = __builtin_amdgcn_make_buffer_rsrc((void*)wq, 0, D * H * DH * 2, 0x00020000);
    const int qo = (32 * w + (lane >> 3)) * 8192 + head * 256 + qh * 128 + (lane & 7) * 16;
    asm volatile("s_nop 4" ::: "memory");
    ASM_LD4(wqv[0], qo, rq, 0); ASM_LD4(wqv[1], qo, rq, 65536); ASM_LD4(wqv[2], qo, rq, 2 * 65536); ASM_LD4(wqv[3], qo, rq, 3 * 65536);
  }
  if (tid == 0) { L.msflag = 0; L.zcnt = 0; L.zflag = 0; L.qflag = 0; L.rdyflag = 0; L.fin = 0; L.chain[0] = 0; L.chain[1] = 0; L.chain[2] = 0; L.chain[3] = 0; }
  if (tid < 64) ((u32*)L.st1)[tid] = SENT;
  const int zso = lane < 32 ? WS_ZS + (lane >> 2) * 16384 + (grp * 32 + cs) * 64 + (lane & 3) * 16 : WS_END;
  if (w == 15) {
    const int o = lane < 33 ? WS_AS + slot * 1024 + lane * 16 : (lane < 37 && qrole) ? WS_MS + (unit - 32) * 64 + (lane - 33) * 16 : WS_END;
    ST_WS((u32x4{SENT, SENT, SENT, SENT}), o);
    ST_WS((u32x4{SENT, SENT, SENT, SENT}), zso);
  }
  bool paced = false;
  const u32 tl_addr = (u32)(size_t)(LAS u32x4*)&L.tile[w][0];
  const bool early = !mrole || w < (qrole ? NEARLY : NEARLYM);
  u64 Eold;
  asm volatile("s_load_dwordx2 %0, %1, 0x0" : "=s"(Eold) : "s"(ws) : "memory");
  if (early) { DMA4(rk, tl_addr, sw * 4096); DMA4(rv, tl_addr + 4096, sw * 4096); }
  asm volatile("s_waitcnt lgkmcnt(0)" : "+s"(Eold) :: "memory");
  const u64 E = Eold + EPOCH_STEP;
  const u32 tag = (u32)E, ehi = (u32)(E >> 32);
  if (mrole) {
    if (early) asm volatile("s_waitcnt vmcnt(8)" ::: "memory"); else asm volatile("s_waitcnt vmcnt(0)" ::: "memory");
    asm volatile("" : "+v"(c0r), "+v"(c1r), "+v"(wm0), "+v"(wm1) :: "memory");
    const float c0 = bflo(c0r), c1 = bflo(c1r);
    const float s0 = rbf(c0 * rcp(1.f + ex2(-c0 * LOG2E))), s1 = rbf(c1 * rcp(1.f + ex2(-c1 * LOG2E)));
    f32x2 a[4];
#pragma unroll
    for (int j = 0; j < 4; ++j) a[j] = f32x2{bflo(wm0[j]), bfhi(wm0[j])} * s0 + f32x2{bflo(wm1[j]), bfhi(wm1[j])} * s1;
    L.red[w][(lane & 4) * 8 + 8 * (lane & 3) + ((lane & 8) >> 1) + r] = red8(a[0][0], a[0][1], a[1][0], a[1][1], a[2][0], a[2][1], a[3][0], a[3][1], b3);
    WG_BARRIER();
    if (w == 15) {
      float p[16];
#pragma unroll
      for (int i = 0; i < 16; ++i) p[i] = L.red[i][lane];
      float v = SUM16(p);
      v = tr32(v, v);
      const u32 pr = pkbf(v, dpp<XOR1>(v));
      const u32 pr2 = asu(dpp<XOR2>(asf(pr)));
      const bool st = lane < 32 && !(lane & 3);
      if (unit < 32) ST_WS((u32x4{pr, tag, pr2, tag}), st ? WS_MG + unit * 128 + lane * 4 : WS_END);
      else __builtin_amdgcn_raw_buffer_store_b64(u32x2{nosent(pr), nosent(pr2)}, rws, st ? WS_MS + (unit - 32) * 64 + lane * 2 : WS_END, 0, AUX_SC1);
    }
    TS(1);
  }
  WG_BARRIER();
  if (qrole) {
    const u32x4 xa = *(const u32x4*)(x + lane * 8);
    const u32x2 xme = *(const u32x2*)(x + 32 * w + 4 * (lane & 7));
    float xv[8];
#pragma unroll
    for (int i = 0; i < 4; ++i) { xv[2 * i] = bflo(xa[i]); xv[2 * i + 1] = bfhi(xa[i]); }
    const float mean = wave_sum(((xv[0] + xv[1]) + (xv[2] + xv[3])) + ((xv[4] + xv[5]) + (xv[6] + xv[7]))) * (1.f / 512.f);
    float qq = 0.f;
#pragma unroll
    for (int i = 0; i < 8; ++i) { const float d = xv[i] - mean; qq = fmaf(d, d, qq); }
    const float mb = rbf(mean), rden = ln_rden(wave_sum(qq) * (1.f / 512.f));
    float xn[4] = {ln_el(bflo(xme[0]), mb, rden), ln_el(bfhi(xme[0]), mb, rden), ln_el(bflo(xme[1]), mb, rden), ln_el(bfhi(xme[1]), mb, rden)};
    asm volatile("" : "+v"(wqv[0]), "+v"(wqv[1]), "+v"(wqv[2]), "+v"(wqv[3]));
    u32 wp[2][8];
#pragma unroll
    for (int i = 0; i < 2; ++i)
#pragma unroll
      for (int j = 0; j < 4; ++j) {
        wp[i][2 * j] = __builtin_amdgcn_perm(wqv[2 * i + 1][j], wqv[2 * i][j], 0x05040100u);
        wp[i][2 * j + 1] = __builtin_amdgcn_perm(wqv[2 * i + 1][j], wqv[2 * i][j], 0x07060302u);
      }
#pragma unroll
    for (int i = 0; i < 2; ++i)
      asm volatile("" : "+v"(wp[i][0]), "+v"(wp[i][1]), "+v"(wp[i][2]), "+v"(wp[i][3]), "+v"(wp[i][4]), "+v"(wp[i][5]), "+v"(wp[i][6]), "+v"(wp[i][7]));
    asm volatile("" : "+v"(xn[0]), "+v"(xn[1]), "+v"(xn[2]), "+v"(xn[3]));
    const int go = lane < 16 ? WS_MG + (lane >> 3) * 2048 + w * 128 + (lane & 7) * 16 : WS_END;
    u32x4 g;
    _Pragma("nounroll") for (u32 spin = 0; spin < SPIN_CAP; ++spin) {
      g = LD_WS(go);
      if ((__ballot(gran_ok(g, tag)) & 0xffffull) == 0xffffull) break;
      __builtin_amdgcn_s_sleep(3);
    }
    TS(1);
    const u32 ca0 = asu(dpp<ROR8>(asf(g[0]))), ca1 = asu(dpp<ROR8>(asf(g[2])));
    const float h0 = rbf(xn[0] * rbf(1.f + bflo(ca0))) + bflo(g[0]), h1 = rbf(xn[1] * rbf(1.f + bfhi(ca0))) + bfhi(g[0]);
    const float h2 = rbf(xn[2] * rbf(1.f + bflo(ca1))) + bflo(g[2]), h3 = rbf(xn[3] * rbf(1.f + bfhi(ca1))) + bfhi(g[2]);
    if (lane < 8) {
      u16* hb = (u16*)&L.hbuf[w][4 * (lane & 1)] + (lane >> 1);
      const u32 p01 = pkbf(h0, h1), p23 = pkbf(h2, h3);
      hb[0] = (u16)p01; hb[4] = (u16)(p01 >> 16); hb[8] = (u16)p23; hb[12] = (u16)(p23 >> 16);
    }
    asm volatile("s_waitcnt lgkmcnt(0)" ::: "memory");
    const u32x2 hp = L.hbuf[w][lane >> 3];
    float a[8];
#pragma unroll
    for (int j = 0; j < 8; ++j) a[j] = dot2(wp[1][j], hp[1], dot2(wp[0][j], hp[0], 0.f));
    L.red[w][8 * (lane & 7) + ((lane & 8) >> 1) + r] = red8(a[0], a[1], a[2], a[3], a[4], a[5], a[6], a[7], b3);
    WG_BARRIER();
    if (w == 15) {
      float p[16];
#pragma unroll
      for (int i = 0; i < 16; ++i) p[i] = L.red[i][lane];
      const float v = SUM16(p);
      const u32 pr = pkbf(v, dpp<XOR1>(v)), pr2 = asu(dpp<XOR2>(asf(pr)));
      ST_WS((u32x4{pr, tag, pr2, tag}), (lane & 3) ? WS_END : WS_QG + head * 512 + qh * 256 + lane * 4);
    }
    TS(2);
    WG_BARRIER();
  }
  if (w == 0) {
    const int go = lane < 32 ? WS_QG + head * 512 + lane * 16 : WS_END;
    u32x4 g = LD_WS(go);
    __builtin_amdgcn_s_sleep(8);
    _Pragma("nounroll") for (u32 spin = 0; spin < SPIN_CAP; ++spin) {
      const u32x4 g2 = LD_WS(go);
      asm volatile("" ::: "memory");
      if ((u32)__ballot(gran_ok(g, tag)) == 0xffffffffu) { if (lane < 32) *(u32x2*)&L.qs[2 * lane] = u32x2{g[0], g[2]}; break; }
      __builtin_amdgcn_s_sleep(2);
      g = LD_WS(go);
      asm volatile("" ::: "memory");
      if ((u32)__ballot(gran_ok(g2, tag)) == 0xffffffffu) { if (lane < 32) *(u32x2*)&L.qs[2 * lane] = u32x2{g2[0], g2[2]}; break; }
      __builtin_amdgcn_s_sleep(2);
    }
    asm volatile("s_waitcnt lgkmcnt(0)" ::: "memory");
    if (lane == 0) L.qflag = 1;
    TS(3);
  }
  paced = w != 0;
  if (!early) { DMA4(rk, tl_addr, sw * 4096); DMA4(rv, tl_addr + 4096, sw * 4096); }
  if (w == 15) {
    asm volatile("s_waitcnt vmcnt(8)" ::: "memory");
    if (lane == 0) __hip_atomic_store((gu64*)(ws + WS_RDY) + slot, E, __ATOMIC_RELAXED, __HIP_MEMORY_SCOPE_AGENT);
  }
  const __amdgpu_buffer_rsrc_t rwo = __builtin_amdgcn_make_buffer_rsrc((void*)wo, 0, H * DH * D * 2, 0x00020000);
  const int woo = w < 4 ? ((grp * 4 + w) * 128 + 8 * (lane >> 2) + 4 * ((lane >> 1) & 1)) * (2 * D) + 32 * cs + 16 * (lane & 1) : H * DH * D * 2;
  u32x4 wov[4];
#pragma unroll
  for (int ks = 0; ks < 4; ++ks) wov[ks] = __builtin_amdgcn_raw_buffer_load_b128(rwo, woo, ks * (2 * D), AUXWO);
  const int dd = 64 * (w & 7) + 4 * c + r;
  u32x4 sm;
  sm[0] = __builtin_amdgcn_raw_buffer_load_b32(__builtin_amdgcn_make_buffer_rsrc((void*)w1, 0, F * 2, 0x00020000), tid * 4, 0, 0);
  sm[1] = __builtin_amdgcn_raw_buffer_load_b32(__builtin_amdgcn_make_buffer_rsrc((void*)b1, 0, F * 2, 0x00020000), tid * 4, 0, 0);
  sm[2] = __builtin_amdgcn_raw_buffer_load_b32(__builtin_amdgcn_make_buffer_rsrc((void*)w2, 0, F * 2, 0x00020000), tid * 4, 0, 0);
  sm[3] = __builtin_amdgcn_raw_buffer_load_b16(__builtin_amdgcn_make_buffer_rsrc((void*)(w ? x : b2), 0, w ? D * 2 : 2, 0x00020000), w ? dd * 2 : 0, 0, 0);
  asm volatile("" ::: "memory");
  LDP(kA, rk, 16 + sw, PACE); LDP(vA, rv, 16 + sw, PACE); LDP(kB, rk, 32 + sw, PACEB); LDP(vB, rv, 32 + sw, PACEB);
  _Pragma("nounroll") for (u32 spin = 0; spin < (1u << 22); ++spin) {
    if (__hip_atomic_load(&L.qflag, __ATOMIC_RELAXED, __HIP_MEMORY_SCOPE_WORKGROUP)) break;
    __builtin_amdgcn_s_sleep(1);
  }
  asm volatile("" ::: "memory");
  if (w != 0) TS(3);
  const u32x4 qp = *(const u32x4*)&L.qs[4 * c];

  float m = -INFINITY, l = 0.f;
  f32x2 acc[4] = {{0.f, 0.f}, {0.f, 0.f}, {0.f, 0.f}, {0.f, 0.f}};
  const bool c8 = c & 8, c4 = c & 4;
#define PIN() do { asm volatile("" ::: "memory"); __builtin_amdgcn_sched_barrier(0); } while (0)
#define QK(xx, kk) do { \
    _Pragma("unroll") for (int t_ = 0; t_ < 4; ++t_) { float d_ = dot2(kk[t_][0], qp[0], 0.f); d_ = dot2(kk[t_][1], qp[1], d_); d_ = dot2(kk[t_][2], qp[2], d_); xx[t_] = dot2(kk[t_][3], qp[3], d_); } \
    asm volatile("" : "+v"(xx[0]), "+v"(xx[1]), "+v"(xx[2]), "+v"(xx[3])); } while (0)
#define SMPV(x_, vv) do { \
    const float y0_ = (c8 ? x_[2] : x_[0]) + dpp<ROR8>(c8 ? x_[0] : x_[2]); \
    const float y1_ = (c8 ? x_[3] : x_[1]) + dpp<ROR8>(c8 ? x_[1] : x_[3]); \
    float z_ = (c4 ? y1_ : y0_) + dpp<HMIR>(c4 ? y0_ : y1_); \
    z_ += dpp<XOR2>(z_); z_ += dpp<XOR1>(z_); \
    const float s_ = rbf(rbf(z_) * SCALE); \
    float mx_ = fmax2(s_, dpp<HMIR>(s_)); mx_ = fmax2(mx_, dpp<ROR8>(mx_)); \
    const float mn_ = fmax2(m, mx_); \
    const float al_ = ex2((m - mn_) * LOG2E), p_ = ex2((s_ - mn_) * LOG2E); \
    m = mn_; l = fmaf(l, al_, p_); \
    _Pragma("unroll") for (int j_ = 0; j_ < 4; ++j_) acc[j_] *= al_; \
    { const float p0_ = dpp<NBC + 0>(p_), p1_ = dpp<NBC + 4>(p_), p2_ = dpp<NBC + 8>(p_), p3_ = dpp<NBC + 12>(p_); \
      _Pragma("unroll") for (int j_ = 0; j_ < 4; ++j_) { \
        acc[j_] += f32x2{bflo(vv[0][j_]), bfhi(vv[0][j_])} * p0_; acc[j_] += f32x2{bflo(vv[1][j_]), bfhi(vv[1][j_])} * p1_; \
        acc[j_] += f32x2{bflo(vv[2][j_]), bfhi(vv[2][j_])} * p2_; acc[j_] += f32x2{bflo(vv[3][j_]), bfhi(vv[3][j_])} * p3_; } } \
    asm volatile("" : "+v"(acc[0]), "+v"(acc[1]), "+v"(acc[2]), "+v"(acc[3]), "+v"(l), "+v"(m)); } while (0)
#define TILE(kk, vv, tn) do { float x_[4]; QK(x_, kk); PIN(); LDT(kk, rk, tn); PIN(); SMPV(x_, vv); PIN(); LDT(vv, rv, tn); PIN(); } while (0)
#define TILE_LAST(kk, vv) do { float x_[4]; QK(x_, kk); PIN(); SMPV(x_, vv); PIN(); } while (0)
  TILE(kA, vA, 48 + sw);
  {
    float x_[4];
    QK(x_, kB); PIN();
#pragma unroll
    for (int i = 0; i < 4; ++i) kB[i] = L.tile[w][i * 64 + lane];
    PIN();
    SMPV(x_, vB); PIN();
#pragma unroll
    for (int i = 0; i < 4; ++i) vB[i] = L.tile[w][256 + i * 64 + lane];
    PIN();
    if (w < 4) {
      u16* wf = (u16*)&L.tile[w][(lane >> 4) * 16 + (lane & 1) * 8] + ((lane >> 1) & 7);
#pragma unroll
      for (int ks = 0; ks < 4; ++ks)
#pragma unroll
        for (int j = 0; j < 4; ++j) { wf[512 * ks + 16 * j] = (u16)(wov[ks][j] & 0xffffu); wf[512 * ks + 16 * j + 8] = (u16)(wov[ks][j] >> 16); }
    }
    L.tile[w][256 + lane] = sm;
    PIN();
  }
  TS(11);
  int t = 48 + sw;
  for (; t + 32 < ntile; t += 32) {
    TILE(kB, vB, t + 16);
    TILE(kA, vA, t + 32);
  }
  TILE(kB, vB, t + 16);
  if (t < ntile) TILE_LAST(kA, vA);
  if (t + 16 < ntile) TILE_LAST(kB, vB);
  TS(4);
  {
    __builtin_amdgcn_s_setprio(3);
    float lr = l + dpp<HMIR>(l); lr += dpp<ROR8>(lr);
    float Mw = xrow_max(m);
    const float sc = ex2((m - Mw) * LOG2E);
    lr = xrow_sum(lr * sc);
#pragma unroll
    for (int j = 0; j < 4; ++j) acc[j] *= sc;
    f32x2 o = {rows4(acc[0][0], acc[0][1], acc[1][0], acc[1][1]), rows4(acc[2][0], acc[2][1], acc[3][0], acc[3][1])};
    const int k4 = w & 3, sl8 = w >= 12 ? w - 8 : k4;
    if (w >= 4 && w < 12) {
      LDS_WAIT(L.chain[k4], (u32)(w >> 2));
      const f32x2 ro = L.run[k4][lane], rml = L.runml[k4];
      const float M = fmax2(Mw, rml[0]), wa = ex2((Mw - M) * LOG2E), wb = ex2((rml[0] - M) * LOG2E);
      o = o * wa + ro * wb;
      lr = lr * wa + rml[1] * wb;
      Mw = M;
    }
    L.run[sl8][lane] = o;
    L.runml[sl8] = f32x2{Mw, lr};
    u32 last = 0;
    f32x2 rr[8], mlv;
    if (w < 8) LDS_SIGNAL(L.chain[k4]);
    else {
      LDS_TICKET(last, L.fin);
      mlv = L.runml[lane & 7];
#pragma unroll
      for (int i = 0; i < 8; ++i) rr[i] = L.run[i][lane];
      LGKM0(last);
      last = __builtin_amdgcn_readfirstlane(last);
    }
    if (last == 7) {
      Mw = fmax2(mlv[0], dpp<XOR1>(mlv[0])); Mw = fmax2(Mw, dpp<XOR2>(Mw)); Mw = fmax2(Mw, dpp<HMIR>(Mw));
      const float wt = ex2((mlv[0] - Mw) * LOG2E);
      lr = sum8(mlv[1] * wt);
      o = ((rr[0] * dpp<NBC + 0>(wt) + rr[1] * dpp<NBC + 1>(wt)) + (rr[2] * dpp<NBC + 2>(wt) + rr[3] * dpp<NBC + 3>(wt))) +
          ((rr[4] * dpp<NBC + 4>(wt) + rr[5] * dpp<NBC + 5>(wt)) + (rr[6] * dpp<NBC + 6>(wt) + rr[7] * dpp<NBC + 7>(wt)));
      const u32 hi = nosent(pkbf(o[0], o[1])), lo = nosent(pkbf(o[0] - bflo(hi), o[1] - bfhi(hi)));
      const float sel = asf((lane & 1) ? hi : lo), own = asf((lane & 1) ? lo : hi);
      const float nb = dpp<XOR1>(sel);
      const float e0 = (lane & 1) ? nb : own, e1 = (lane & 1) ? own : nb;
      const u32 f0 = asu(dpp<XOR2>(e0)), f1 = asu(dpp<XOR2>(e1));
      const bool ml = lane == 2;
      const u32x4 pc = {ml ? nosent(asu(Mw)) : asu(e0), ml ? nosent(asu(lr)) : asu(e1), f0, f1};
      ST_WS(pc, ml ? WS_AS + slot * 1024 + 512 : (lane & 2) ? WS_END : WS_AS + slot * 1024 + (lane & 1) * 256 + (lane >> 2) * 16);
      TS(10);
    }
  }
  __builtin_amdgcn_s_setprio(0);
  TS(5);
  const u32x4 smv = L.tile[w][256 + lane];
#define PARK() do { _Pragma("nounroll") for (u32 spin_ = 0; spin_ < (1u << 16); ++spin_) { \
    if (__hip_atomic_load(&L.fin, __ATOMIC_RELAXED, __HIP_MEMORY_SCOPE_WORKGROUP) >= 5u) break; \
    __builtin_amdgcn_s_sleep(GSLEEP); } } while (0)
  if (w != 4) PARK();
  if (w == 4) {
    _Pragma("nounroll") for (u32 spin = 0; spin < SPIN_CAP; ++spin) {
      const u32x4 rd0 = LD_WS(WS_RDY + lane * 16), rd1 = LD_WS(WS_RDY + 1024 + lane * 16);
      if (__all(((rd0[0] ^ tag) | (rd0[2] ^ tag) | (rd1[0] ^ tag) | (rd1[2] ^ tag) | (rd0[1] ^ ehi) | (rd0[3] ^ ehi) | (rd1[1] ^ ehi) | (rd1[3] ^ ehi)) == 0u)) break;
      __builtin_amdgcn_s_sleep(8);
    }
    if (lane == 0) L.rdyflag = 1;
    u32x4 mv[4];
    _Pragma("nounroll") for (u32 spin = 0; spin < SPIN_CAP; ++spin) {
#pragma unroll
      for (int i = 0; i < 4; ++i) mv[i] = LD_WS(WS_MS + i * 1024 + lane * 16);
      if (__all(max(max(max4(mv[0]), max4(mv[1])), max(max4(mv[2]), max4(mv[3]))) != SENT)) break;
      __builtin_amdgcn_s_sleep(8);
    }
#pragma unroll
    for (int i = 0; i < 4; ++i) L.ms[i * 64 + lane] = mv[i];
    LDS_SIGNAL(L.msflag);
    PARK();
  } else if (w < 4) {
    u32x4 bfr[4];
#pragma unroll
    for (int ks = 0; ks < 4; ++ks) bfr[ks] = L.tile[w][64 * ks + lane];
#define LDS_WAIT_SLEEP(word, n) do { _Pragma("nounroll") for (u32 spin_ = 0; spin_ < (1u << 20); ++spin_) { \
    if (__hip_atomic_load(&(word), __ATOMIC_RELAXED, __HIP_MEMORY_SCOPE_WORKGROUP) >= (n)) break; \
    __builtin_amdgcn_s_sleep(2); } asm volatile("" ::: "memory"); } while (0)
    LDS_WAIT_SLEEP(L.rdyflag, 1u);
    __builtin_amdgcn_s_setprio(2);
    const int po = WS_AS + ((grp * 4 + w) * 8 + (lane & 7)) * 1024;
    const int ao = po + (lane & 8) * 32 + r * 16;
    u32x4 af[4];
#pragma unroll
    for (int ks = 0; ks < 4; ++ks) af[ks] = u32x4{SENT, SENT, SENT, SENT};
    u32x2 mlp = {SENT, SENT};
    u64 miss = ~0ull;
    u32 seen = 0;
    _Pragma("nounroll") for (u32 spin = 0; spin < SPIN_CAP; ++spin) {
      const bool few = __builtin_popcount(seen) >= 6;
      const u32 rep = few ? ~0u : seen * 0x01010101u;
      const u64 want = miss & (((u64)rep << 32) | rep);
      if (__builtin_amdgcn_inverse_ballot_w64(want)) {
#pragma unroll
        for (int ks = 0; ks < 4; ++ks) af[ks] = LD_WS(ao + 64 * ks);
      }
      if (__builtin_amdgcn_inverse_ballot_w64(want | (miss & 0xffull))) mlp = __builtin_amdgcn_raw_buffer_load_b64(rws, po + 512, 0, AUX_SC1);
      const u32 mm = max(mlp[0], mlp[1]);
      miss = __ballot(max(max(max4(af[0]), max4(af[1])), max(max(max4(af[2]), max4(af[3])), mm)) == SENT);
      if (!miss) break;
      seen = (u32)__ballot(mm != SENT) & 0xffu;
      if (few) __builtin_amdgcn_s_sleep(1); else __builtin_amdgcn_s_sleep(4);
    }
    TS(6);
    f32x4 d4 = {0.f, 0.f, 0.f, 0.f};
#pragma unroll
    for (int ks = 0; ks < 4; ++ks) d4 = __builtin_amdgcn_mfma_f32_16x16x32_bf16(__builtin_bit_cast(bf16x8, af[ks]), __builtin_bit_cast(bf16x8, bfr[ks]), d4, 0, 0, 0);
    const float Ms = asf(mlp[0]);
    float Mx = fmax2(Ms, dpp<XOR1>(Ms)); Mx = fmax2(Mx, dpp<XOR2>(Mx)); Mx = fmax2(Mx, dpp<HMIR>(Mx));
    const float wt = ex2((Ms - Mx) * LOG2E);
    const float cf = wt * rcp(sum8(wt * asf(mlp[1])));
    const float zp = xrow_sum(fmaf(dpp_evod<NBC + 0, NBC + 4>(cf), d4[0], dpp_evod<NBC + 1, NBC + 5>(cf) * d4[1]) +
                              fmaf(dpp_evod<NBC + 2, NBC + 6>(cf), d4[2], dpp_evod<NBC + 3, NBC + 7>(cf) * d4[3]));
    L.zpart[w][c] = zp;
    u32 last;
    LDS_TICKET(last, L.zcnt);
    const f32x4 z0 = *(const f32x4*)&L.zpart[0][4 * (lane & 3)], z1 = *(const f32x4*)&L.zpart[1][4 * (lane & 3)];
    const f32x4 z2 = *(const f32x4*)&L.zpart[2][4 * (lane & 3)], z3 = *(const f32x4*)&L.zpart[3][4 * (lane & 3)];
    LGKM0(last);
    if (__builtin_amdgcn_readfirstlane(last) == 3) {
      const f32x4 z = (z0 + z1) + (z2 + z3);
      const u32x4 zz = {nosent(asu(z[0])), nosent(asu(z[1])), nosent(asu(z[2])), nosent(asu(z[3]))};
      ST_WS(zz, zso);
      L.zflag = 1;
    }
    __builtin_amdgcn_s_setprio(0);
    TS(7);
  } else if (w >= 8) {
    LDS_WAIT_SLEEP(L.msflag, 1u);
    const u16* msv = (const u16*)L.ms + dd;
    const float xv = bflo(smv[3]), ga = bf2f(msv[0]);
    float xsb = bf2f(msv[512]), xcb = rbf(1.f + bf2f(msv[1024]));
    asm volatile("" : "+v"(xsb), "+v"(xcb));
    const int zo = WS_ZS + (bid & 7) * 16384 + ((lane >> 3) * 32 + 4 * (w & 7)) * 64 + (lane & 7) * 16;
    u32x4 z0 = {SENT, SENT, SENT, SENT}, z1 = z0;
    u64 miss = ~0ull, want = ~0ull;
    LDS_WAIT_SLEEP(L.zflag, 1u);
    __builtin_amdgcn_s_setprio(3);
    _Pragma("nounroll") for (u32 spin = 0; spin < SPIN_CAP; ++spin) {
      if (__builtin_amdgcn_inverse_ballot_w64(want | (miss & 0x0101010101010101ull))) z0 = LD_WS(zo);
      if (__builtin_amdgcn_inverse_ballot_w64(want)) z1 = LD_WS(zo + 128);
      const u32 m0 = max4(z0);
      miss = __ballot(max(m0, max4(z1)) == SENT);
      if (!miss) break;
      const u64 seen = __ballot(m0 != SENT) & 0x0101010101010101ull;
      want = __builtin_popcountll(seen) >= 6 ? miss : miss & (seen * 0xffull);
      __builtin_amdgcn_s_sleep(1);
    }
    TS(6);
    const float at = red8(asf(z0[0]), asf(z0[1]), asf(z0[2]), asf(z0[3]), asf(z1[0]), asf(z1[1]), asf(z1[2]), asf(z1[3]), b3);
    const float x1 = rbf(xv + rbf(ga * rbf(at)));
#define LDS_PUT(slot, v) do { const u32x2 t_ = {nosent(asu(v[0])), nosent(asu(v[1]))}; *(u32x2*)&(slot) = t_; } while (0)
#define LDS_GET(dst, addr, SLP) do { _Pragma("nounroll") for (u32 spin_ = 0; spin_ < (1u << 22); ++spin_) { \
    const u32x2 t_ = __builtin_bit_cast(u32x2, __hip_atomic_load((const u64*)(addr), __ATOMIC_RELAXED, __HIP_MEMORY_SCOPE_WORKGROUP)); \
    dst = f32x2{asf(t_[0]), asf(t_[1])}; if (__all(max(t_[0], t_[1]) != SENT)) break; if (SLP) __builtin_amdgcn_s_sleep(1); } asm volatile("" ::: "memory"); } while (0)
    const f32x2 p1 = ln_part(x1, lane & 1);
    LDS_PUT(L.st1[w - 8], p1);
    f32x2 pa;
    LDS_GET(pa, &L.st1[lane & 7], 0);
    TS(8);
    float mb, rden;
    ln_comb(pa, mb, rden);
    const float hh = rbf(rbf(ln_el(x1, mb, rden) * xcb) + xsb);
    if ((dd >> 1) == bid) { L.x1d[dd & 1] = x1; L.hd[dd & 1] = hh; }
    const f32x2 p2 = ln_part(hh, lane & 1);
    LDS_PUT(L.st2[w - 8], p2);
    __builtin_amdgcn_s_setprio(0);
  }
  if (w < 11) {
    f32x2 w1f = {bflo(smv[0]), bfhi(smv[0])}, b1f = {bflo(smv[1]), bfhi(smv[1])};
    asm volatile("" : "+v"(w1f), "+v"(b1f));
    if (w < 8) LDS_WAIT_SLEEP(L.zflag, 1u);
    f32x2 pa;
    LDS_GET(pa, &L.st2[lane & 7], 1);
    TS(9);
    const f32x2 hd = *(const f32x2*)L.hd;
    float mb, rden;
    ln_comb(pa, mb, rden);
    f32x2 y;
#pragma unroll
    for (int k = 0; k < 2; ++k) {
      const float tt = ln_el(hd[k], mb, rden);
      const f32x2 tw = w1f * tt;
      const u32 twb = pkbf(tw[0], tw[1]);
      const f32x2 us = f32x2{bflo(twb), bfhi(twb)} + b1f;
      const u32 ub = pkbf(us[0], us[1]);
      const f32x2 uu = {bflo(ub), bfhi(ub)};
      constexpr float KK = -2.f * 0.7978845608028654f * LOG2E;
      const f32x2 zz = uu * (uu * uu * (0.044715f * KK) + KK);
      const f32x2 sg = {rcp(1.f + ex2(zz[0])), rcp(1.f + ex2(zz[1]))};
      const f32x2 gg = uu * sg;
      y[k] = dot2(pkbf(gg[0], gg[1]), smv[2], 0.f);
    }
    const float yy = wave_sum2(y[0], y[1], lane & 1);
    if (lane < 2) ((u32*)&L.yred[w])[lane] = nosent(asu(yy));
  }
  if (w == 0) {
    const int k = r & 1;
    const float gbv = bf2f(((const u16*)L.ms)[1536 + 2 * bid + k]), x1v = L.x1d[k];
    f32x2 yv;
    LDS_GET(yv, &L.yred[min(c, 10)], 1);
    float y = c < 11 ? yv[k] : 0.f;
    y = sum8(y); y += dpp<ROR8>(y);
    const float yy = rbf(rbf(y) + bflo(smv[3]));
    const u32 o16 = pkbf(x1v + rbf(gbv * yy), 0.f);
    if (lane == 0 || lane == 16) out[2 * bid + k] = (u16)o16;
    if (bid == 0 && lane == 0) __hip_atomic_store((gu64*)(ws + WS_E), E, __ATOMIC_RELAXED, __HIP_MEMORY_SCOPE_AGENT);
  }
}

extern "C" void kernel_launch(void* const* d_in, const int* in_sizes, int n_in, void* d_out, int out_size, void* d_ws, size_t ws_size, hipStream_t stream) {
  fused<<<256, 1024, 0, stream>>>((const u16*)d_in[0], (const u16*)d_in[1], (const u16*)d_in[2], (const u16*)d_in[3], (const u16*)d_in[4],
                                  (const u16*)d_in[6], (const u16*)d_in[7], (const u16*)d_in[8], (const u16*)d_in[9], (const u16*)d_in[10],
                                  (const u16*)d_in[11], (u16*)d_out, (unsigned char*)d_ws);
}
```
